```python
import torch

def _softmax(x, axis=-1):
    return torch.softmax(x, dim=axis)
def _gelu(x):
    return torch.nn.functional.gelu(x, approximate="tanh")
def _silu(x):
    return torch.nn.functional.silu(x)
def _sigmoid(x):
    return torch.sigmoid(x)
def _layernorm(x, axis=-1, eps=1e-5):
    m = x.mean(dim=axis, keepdim=True)
    v = x.var(dim=axis, keepdim=True, unbiased=False)
    return (x - m) / torch.sqrt(v + eps)
def _scatter_add(tgt, idx, src, axis=0):
    return tgt.clone().index_add_(axis, idx, src)
def _segment_sum(src, seg, n):
    out = torch.zeros((n,) + src.shape[1:], dtype=src.dtype)
    return out.index_add_(0, seg, src)
def _pad(x, pw):
    flat = tuple(v for pair in reversed(pw) for v in pair)
    return torch.nn.functional.pad(x, flat)

kernel_name = 'recurrent_scan__ssm_conv_s5__geglu_ffn__prompt'

def setup_inputs():

    _w = (lambda a: (lambda t: t.to(torch.bfloat16) if t.is_floating_point() else t)(torch.as_tensor(a)))
    def _A_setup():
        B, S, D = 1, 2048, 1024
        P = D
        Lambda_re = -0.5 * (lambda _x: torch.exp(torch.as_tensor(_x)))((lambda *s: torch.randn(*s, dtype=torch.bfloat16) * 0.2)(P))
        Lambda_im = (lambda *s: torch.randn(*s, dtype=torch.bfloat16) * 0.4)(P) * 1.0
        B_re = (lambda *s: torch.randn(*s, dtype=torch.bfloat16) * 0.2)(P, D) * (D ** -0.5)
        B_im = (lambda *s: torch.randn(*s, dtype=torch.bfloat16) * 0.2)(P, D) * (D ** -0.5)
        C_re = (lambda *s: torch.randn(*s, dtype=torch.bfloat16) * 0.2)(D, P) * (P ** -0.5)
        C_im = (lambda *s: torch.randn(*s, dtype=torch.bfloat16) * 0.2)(D, P) * (P ** -0.5)
        log_dt = (lambda *s: torch.randn(*s, dtype=torch.bfloat16) * 0.2)(P) * 0.3 - 2.0
        Dskip = (lambda *s: torch.randn(*s, dtype=torch.bfloat16) * 0.05)(D)
        return {
            "x": _w((lambda *s: torch.randn(*s, dtype=torch.bfloat16) * 0.05)(B, S, D)),
            "Lambda_re": _w(Lambda_re),
            "Lambda_im": _w(Lambda_im),
            "B_re": _w(B_re),
            "B_im": _w(B_im),
            "C_re": _w(C_re),
            "C_im": _w(C_im),
            "log_dt": _w(log_dt),
            "Dskip": _w(Dskip),
        }
    _A_in = _A_setup()

    def _B_setup():
        B, S, D, F = 1, 2048, 1024, 3584
        return {
            "x": _w((lambda *s: torch.randn(*s, dtype=torch.bfloat16) * 0.05)(B, S, D)),
            "w_gate": _w((lambda *s: torch.randn(*s, dtype=torch.bfloat16) * 0.05)(D, F)),
            "w_up": _w((lambda *s: torch.randn(*s, dtype=torch.bfloat16) * 0.05)(D, F)),
            "w_down": _w((lambda *s: torch.randn(*s, dtype=torch.bfloat16) * 0.05)(F, D)),
        }
    _B_in = _B_setup()

    return {
        "A_x": _A_in["x"],
        "A_Lambda_re": _A_in["Lambda_re"],
        "A_Lambda_im": _A_in["Lambda_im"],
        "A_B_re": _A_in["B_re"],
        "A_B_im": _A_in["B_im"],
        "A_C_re": _A_in["C_re"],
        "A_C_im": _A_in["C_im"],
        "A_log_dt": _A_in["log_dt"],
        "A_Dskip": _A_in["Dskip"],
        "B_x": _B_in["x"],
        "B_w_gate": _B_in["w_gate"],
        "B_w_up": _B_in["w_up"],
        "B_w_down": _B_in["w_down"],
    }

def reference(A_x, A_Lambda_re, A_Lambda_im, A_B_re, A_B_im, A_C_re, A_C_im, A_log_dt, A_Dskip, B_x, B_w_gate, B_w_up, B_w_down):
    def _A_ref(x, Lambda_re, Lambda_im, B_re, B_im, C_re, C_im, log_dt, Dskip):
        Bsz, L, H = x.shape
        Lam = Lambda_re + 1j * Lambda_im
        Bmat = B_re + 1j * B_im
        Cmat = C_re + 1j * C_im
        dt = torch.exp(log_dt)
        Lam_bar = torch.exp(dt * Lam)
        B_bar = ((Lam_bar - 1.0) / Lam)[:, None] * Bmat
        Bu = torch.einsum("bld,pd->blp", x + 0j, B_bar)
        h = torch.zeros_like(Bu[:, 0, :])
        hs = []
        for t in range(L):
            h = Lam_bar[None, :] * h + Bu[:, t, :]
            hs.append(h)
        hseq = torch.stack(hs, 1)
        y = 2.0 * torch.einsum("blp,dp->bld", hseq, Cmat).real + x * Dskip[None, None, :]
        return y

    def _B_ref(x, w_gate, w_up, w_down):
        return (_gelu(x @ w_gate) * (x @ w_up)) @ w_down

    x = A_x
    xn = _layernorm(x, axis=-1)
    s = _A_ref(xn, A_Lambda_re, A_Lambda_im, A_B_re, A_B_im, A_C_re, A_C_im, A_log_dt, A_Dskip)
    g = _silu(_B_ref(xn, B_w_gate, B_w_up, B_w_down))
    return x + s * g
```

```cpp
#include <hip/hip_runtime.h>
#include <hip/hip_bf16.h>
#include <stdint.h>

typedef __hip_bfloat16 bf16;
typedef __attribute__((ext_vector_type(8))) short bf16x8;
typedef __attribute__((ext_vector_type(4))) short bf16x4;
typedef __attribute__((ext_vector_type(4))) float f32x4;
typedef __attribute__((__vector_size__(4 * sizeof(short)))) short v4s;

constexpr int L_SEQ = 2048, DM = 1024, FF = 3584, PP = 1024;
constexpr int LDW = 3584;
constexpr int KAH = FF + 2 * PP;
constexpr int SCAN_T = 32, NCHUNK = L_SEQ / SCAN_T;

constexpr size_t OFF_XN   = 0;
constexpr size_t OFF_BT   = OFF_XN + (size_t)L_SEQ * DM * 2;
constexpr size_t OFF_CT   = OFF_BT + (size_t)DM * LDW * 2;
constexpr size_t OFF_AH   = OFF_CT + (size_t)2 * PP * DM * 2;
constexpr size_t OFF_BU   = OFF_AH + (size_t)L_SEQ * KAH * 2;
constexpr size_t OFF_E    = OFF_BU + (size_t)L_SEQ * PP * 8;
constexpr size_t OFF_LAM  = OFF_E + (size_t)NCHUNK * PP * 8;
constexpr size_t OFF_COEF = OFF_LAM + (size_t)PP * 8;

__device__ __forceinline__ float bf2f(unsigned short u) { return __uint_as_float(((unsigned)u) << 16); }
__device__ __forceinline__ unsigned short f2bf(float f) {
  unsigned u = __float_as_uint(f);
  if ((u & 0x7fffffffu) > 0x7f800000u) return (unsigned short)((u >> 16) | 0x40);
  u += 0x7fffu + ((u >> 16) & 1u);
  return (unsigned short)(u >> 16);
}
__device__ __forceinline__ float rbf(float f) { return bf2f(f2bf(f)); }

constexpr int PREP_LN_BLOCKS = L_SEQ / 4;
constexpr int PREP_TR_BLOCKS = 4 * (1024 / 64) * (1024 / 64);
constexpr int PREP_BLOCKS = PREP_LN_BLOCKS + PREP_TR_BLOCKS + 4;

__global__ __launch_bounds__(256) void prep_kernel(
    const bf16* __restrict__ x, const bf16* __restrict__ lam_re, const bf16* __restrict__ lam_im,
    const bf16* __restrict__ b_re, const bf16* __restrict__ b_im,
    const bf16* __restrict__ c_re, const bf16* __restrict__ c_im,
    const bf16* __restrict__ log_dt, char* __restrict__ ws) {
  __shared__ unsigned short tile[64][66];
  const int bid = blockIdx.x, tid = threadIdx.x;
  if (bid < PREP_LN_BLOCKS) {
    const int wave = tid >> 6, lane = tid & 63;
    const int row = bid * 4 + wave;
    const unsigned short* xr = (const unsigned short*)x + (size_t)row * DM;
    float v[16];
    bf16x8 a = *(const bf16x8*)(xr + lane * 8);
    bf16x8 b = *(const bf16x8*)(xr + 512 + lane * 8);
    float sum = 0.f;
#pragma unroll
    for (int j = 0; j < 8; ++j) { v[j] = bf2f((unsigned short)a[j]); v[8 + j] = bf2f((unsigned short)b[j]); }
#pragma unroll
    for (int j = 0; j < 16; ++j) sum += v[j];
#pragma unroll
    for (int o = 32; o > 0; o >>= 1) sum += __shfl_xor(sum, o);
    const float mean = sum * (1.0f / DM);
    float sq = 0.f;
#pragma unroll
    for (int j = 0; j < 16; ++j) { float d = v[j] - mean; sq += d * d; }
#pragma unroll
    for (int o = 32; o > 0; o >>= 1) sq += __shfl_xor(sq, o);
    const float var_b = rbf(sq * (1.0f / DM));
    const float mean_b = rbf(mean);
    const float den = rbf(sqrtf(rbf(var_b + 1e-5f)));
    bf16x8 oa, ob;
#pragma unroll
    for (int j = 0; j < 8; ++j) {
      oa[j] = (short)f2bf(rbf(v[j] - mean_b) / den);
      ob[j] = (short)f2bf(rbf(v[8 + j] - mean_b) / den);
    }
    unsigned short* xo = (unsigned short*)(ws + OFF_XN) + (size_t)row * DM;
    *(bf16x8*)(xo + lane * 8) = oa;
    *(bf16x8*)(xo + 512 + lane * 8) = ob;
  } else if (bid < PREP_LN_BLOCKS + PREP_TR_BLOCKS) {
    const int t = bid - PREP_LN_BLOCKS;
    const int mat = t >> 8, ti = (t >> 4) & 15, tj = t & 15;
    const unsigned short* src = (const unsigned short*)(mat == 0 ? b_re : mat == 1 ? b_im : mat == 2 ? c_re : c_im);
    const int tx = tid & 63, ty = tid >> 6;
#pragma unroll
    for (int r = ty; r < 64; r += 4) tile[r][tx] = src[(size_t)(ti * 64 + r) * 1024 + tj * 64 + tx];
    __syncthreads();
    unsigned short* dst; size_t ld; unsigned short neg = 0;
    if (mat < 2) { dst = (unsigned short*)(ws + OFF_BT) + mat * 1024; ld = LDW; }
    else { dst = (unsigned short*)(ws + OFF_CT) + (size_t)(mat - 2) * 1024 * DM; ld = DM; neg = (mat == 3) ? 0x8000 : 0; }
#pragma unroll
    for (int r = ty; r < 64; r += 4) dst[(size_t)(tj * 64 + r) * ld + ti * 64 + tx] = tile[tx][r] ^ neg;
  } else {
    const int p = (bid - PREP_LN_BLOCKS - PREP_TR_BLOCKS) * 256 + tid;
    const float lr = bf2f(((const unsigned short*)lam_re)[p]);
    const float li = bf2f(((const unsigned short*)lam_im)[p]);
    const float dt = rbf(expf(bf2f(((const unsigned short*)log_dt)[p])));
    const float ar = dt * lr, ai = dt * li;
    const float e = expf(ar);
    float sn, cs; sincosf(ai, &sn, &cs);
    const float br_ = e * cs, bi_ = e * sn;
    const float nr = br_ - 1.0f, ni = bi_;
    const float den = lr * lr + li * li;
    const float cr = (nr * lr + ni * li) / den, ci = (ni * lr - nr * li) / den;
    ((float2*)(ws + OFF_LAM))[p] = make_float2(br_, bi_);
    ((float2*)(ws + OFF_COEF))[p] = make_float2(cr, ci);
  }
}

__device__ __forceinline__ int a_lds_byte(int r, int c) {
  int st = (r >> 4) * 2 + (c >> 5), rr = r & 15, cc = c & 31, ob = rr * 64 + cc * 2;
  return st * 1024 + (ob ^ (((ob >> 9) & 1) << 5));
}
__device__ __forceinline__ void a_stage_rc(int b, int& R, int& C) {
  int st = b >> 10, sb = b & 1023, swz = sb ^ (((sb >> 9) & 1) << 5);
  R = (st >> 1) * 16 + (swz >> 6); C = (st & 1) * 32 + ((swz & 63) >> 1);
}
__device__ __forceinline__ bf16x8 tr_frag(const char* lds_lo, const char* lds_hi) {
  v4s lo = __builtin_amdgcn_ds_read_tr16_b64_v4i16((__attribute__((address_space(3))) v4s*)(lds_lo));
  v4s hi = __builtin_amdgcn_ds_read_tr16_b64_v4i16((__attribute__((address_space(3))) v4s*)(lds_hi));
  bf16x8 r;
  r[0] = lo[0]; r[1] = lo[1]; r[2] = lo[2]; r[3] = lo[3];
  r[4] = hi[0]; r[5] = hi[1]; r[6] = hi[2]; r[7] = hi[3];
  return r;
}
#define GLDS16(gptr, ldsptr) __builtin_amdgcn_global_load_lds((const void __attribute__((address_space(1)))*)(gptr), (void __attribute__((address_space(3)))*)(ldsptr), 16, 0, 0)

__device__ __forceinline__ float gelu_tanh_f(float x) {
  const float k0 = 0.7978845608028654f, k1 = 0.044715f;
  float u = k0 * (x + k1 * x * x * x);
  return 0.5f * x * (1.0f + tanhf(u));
}

constexpr int G1_BM = 256, G1_BN = 288, G1_CH = G1_BN / 8;
constexpr int G1_A_BYTES = G1_BM * 64 * 2, G1_B_BYTES = 64 * G1_BN * 2, G1_STAGE = G1_A_BYTES + G1_B_BYTES;
__device__ __forceinline__ int g1_swz(int k) { return ((k >> 3) & 1) << 1; }

__global__ __launch_bounds__(512, 2) void gemm1_kernel(
    const bf16* __restrict__ wg, const bf16* __restrict__ wu, char* __restrict__ ws) {
  extern __shared__ __attribute__((aligned(16))) char smem[];
  const int tid = threadIdx.x, lane = tid & 63, wid = tid >> 6;
  const int bid = blockIdx.x;
  const int pm = bid & 7, pn = bid >> 3;
  const int brow = pm * G1_BM;
  const unsigned short* xn = (const unsigned short*)(ws + OFF_XN);
  const unsigned short* bt = (const unsigned short*)(ws + OFF_BT);

  unsigned aoffs[4];
#pragma unroll
  for (int i = 0; i < 4; ++i) {
    int b = (i * 8 + wid) * 1024 + lane * 16, R, C; a_stage_rc(b, R, C);
    aoffs[i] = (unsigned)((brow + R) * DM + C);
  }
  const unsigned short* bsrc[5];
#pragma unroll
  for (int i = 0; i < 5; ++i) {
    int inst = i * 8 + wid; if (inst > 35) inst = 35;
    int Lc = inst * 64 + lane, k = Lc / G1_CH, cp = Lc % G1_CH, c = cp ^ g1_swz(k);
    int half = c >= 18, cc = c - half * 18, grp = pn * 9 + (cc >> 1), col = (cc & 1) * 8;
    const unsigned short* base = (const unsigned short*)(half ? wu : wg);
    int o = grp * 16;
    if (grp >= 224) { base = bt; o = half * 1024 + (grp - 224) * 16; }
    bsrc[i] = base + (k * LDW + col + o);
  }
#define G1_STAGE_LOADS(buf, kt) do { \
    char* sa_ = smem + (buf) * G1_STAGE; char* sb_ = sa_ + G1_A_BYTES; \
    _Pragma("unroll") for (int i = 0; i < 4; ++i) GLDS16(xn + aoffs[i] + (kt) * 64, sa_ + (i * 8 + wid) * 1024); \
    _Pragma("unroll") for (int i = 0; i < 4; ++i) GLDS16(bsrc[i] + (size_t)(kt) * 64 * LDW, sb_ + (i * 8 + wid) * 1024); \
    if (wid < 4) GLDS16(bsrc[4] + (size_t)(kt) * 64 * LDW, sb_ + (32 + wid) * 1024); \
  } while (0)
  f32x4 acc[2][18];
#pragma unroll
  for (int i = 0; i < 2; ++i)
#pragma unroll
    for (int j = 0; j < 18; ++j) acc[i][j] = (f32x4){0.f, 0.f, 0.f, 0.f};

  const int fr = lane & 15, fq = lane >> 4, tq = (lane & 15) >> 2, tp = lane & 3;
  int aoff[2][2];
#pragma unroll
  for (int mi = 0; mi < 2; ++mi)
#pragma unroll
    for (int kk = 0; kk < 2; ++kk) aoff[mi][kk] = a_lds_byte(wid * 32 + mi * 16 + fr, kk * 32 + fq * 8);
  const int bRow = ((8 * fq + tq) * G1_CH + (tp >> 1)) * 16 + 8 * (tp & 1);
  const int bEven = bRow + 32 * (fq & 1), bOdd = bRow - 32 * (fq & 1);

  G1_STAGE_LOADS(0, 0);
  __syncthreads();
  for (int kt = 0; kt < 16; ++kt) {
    const int buf = kt & 1;
    if (kt + 1 < 16) G1_STAGE_LOADS(buf ^ 1, kt + 1);
    const char* sa = smem + buf * G1_STAGE; const char* sb = sa + G1_A_BYTES;
#pragma unroll
    for (int kk = 0; kk < 2; ++kk) {
      bf16x8 af[2];
#pragma unroll
      for (int mi = 0; mi < 2; ++mi) af[mi] = *(const bf16x8*)(sa + aoff[mi][kk]);
#pragma unroll
      for (int jg = 0; jg < 3; ++jg) {
        bf16x8 b[6];
#pragma unroll
        for (int jj = 0; jj < 6; ++jj) {
          const int j = jg * 6 + jj;
          const int bo = ((j & 1) ? bOdd : bEven) + kk * 32 * G1_CH * 16 + 32 * j;
          b[jj] = tr_frag(sb + bo, sb + bo + 4 * G1_CH * 16);
        }
#pragma unroll
        for (int jj = 0; jj < 6; ++jj)
#pragma unroll
          for (int mi = 0; mi < 2; ++mi) acc[mi][jg * 6 + jj] = __builtin_amdgcn_mfma_f32_16x16x32_bf16(af[mi], b[jj], acc[mi][jg * 6 + jj], 0, 0, 0);
        asm volatile("" ::: "memory");
      }
    }
    __syncthreads();
  }
  int tz = threadIdx.x; asm volatile("" : "+v"(tz));
  const int wid2 = tz >> 6, fr2 = tz & 15, fq2 = (tz >> 4) & 3;
  unsigned short* ah = (unsigned short*)(ws + OFF_AH);
  float2* bu = (float2*)(ws + OFF_BU);
  const float2* coef = (const float2*)(ws + OFF_COEF);
#pragma unroll
  for (int j = 0; j < 9; ++j) {
    const int grp = pn * 9 + j;
    if (grp < 224) {
      const int col = grp * 16 + fr2;
#pragma unroll
      for (int mi = 0; mi < 2; ++mi)
#pragma unroll
        for (int r = 0; r < 4; ++r) {
          const int row = brow + wid2 * 32 + mi * 16 + fq2 * 4 + r;
          float g = rbf(acc[mi][j][r]), u = rbf(acc[mi][9 + j][r]);
          float a = rbf(gelu_tanh_f(g)) * u;
          ah[(size_t)row * KAH + col] = f2bf(a);
        }
    } else {
      const int p = (grp - 224) * 16 + fr2;
      const float2 cf = coef[p];
#pragma unroll
      for (int mi = 0; mi < 2; ++mi)
#pragma unroll
        for (int r = 0; r < 4; ++r) {
          const int row = brow + wid2 * 32 + mi * 16 + fq2 * 4 + r;
          float ur = acc[mi][j][r], ui = acc[mi][9 + j][r];
          bu[(size_t)row * PP + p] = make_float2(cf.x * ur - cf.y * ui, cf.x * ui + cf.y * ur);
        }
    }
  }
}

__global__ __launch_bounds__(256) void scanA_kernel(char* __restrict__ ws) {
  const int p = (blockIdx.x & 3) * 256 + threadIdx.x, c = blockIdx.x >> 2;
  const float2 lam = ((const float2*)(ws + OFF_LAM))[p];
  const float2* bu = (const float2*)(ws + OFF_BU) + (size_t)c * SCAN_T * PP + p;
  float er = 0.f, ei = 0.f;
#pragma unroll 8
  for (int s = 0; s < SCAN_T; ++s) {
    float2 b = bu[(size_t)s * PP];
    float nr = lam.x * er - lam.y * ei + b.x, ni = lam.x * ei + lam.y * er + b.y;
    er = nr; ei = ni;
  }
  ((float2*)(ws + OFF_E))[(size_t)c * PP + p] = make_float2(er, ei);
}

__global__ __launch_bounds__(256) void scanC_kernel(char* __restrict__ ws) {
  const int p = (blockIdx.x & 3) * 256 + threadIdx.x, c = blockIdx.x >> 2;
  const float2 lam = ((const float2*)(ws + OFF_LAM))[p];
  float lr = lam.x, li = lam.y;
#pragma unroll
  for (int i = 0; i < 5; ++i) { float a = lr * lr - li * li, b = 2.f * lr * li; lr = a; li = b; }
  const float2* E = (const float2*)(ws + OFF_E) + p;
  float hr = 0.f, hi = 0.f;
  for (int cc = 0; cc < c; ++cc) {
    float2 e = E[(size_t)cc * PP];
    float nr = lr * hr - li * hi + e.x, ni = lr * hi + li * hr + e.y;
    hr = nr; hi = ni;
  }
  const float2* bu = (const float2*)(ws + OFF_BU) + (size_t)c * SCAN_T * PP + p;
  unsigned short* ah = (unsigned short*)(ws + OFF_AH) + (size_t)c * SCAN_T * KAH + FF + p;
#pragma unroll 8
  for (int s = 0; s < SCAN_T; ++s) {
    float2 b = bu[(size_t)s * PP];
    float nr = lam.x * hr - lam.y * hi + b.x, ni = lam.x * hi + lam.y * hr + b.y;
    hr = nr; hi = ni;
    ah[(size_t)s * KAH] = f2bf(hr);
    ah[(size_t)s * KAH + PP] = f2bf(hi);
  }
}

constexpr int G2_BM = 128, G2_BN = 64, G2_CH = G2_BN / 8;
constexpr int G2_A_BYTES = G2_BM * 64 * 2, G2_B_BYTES = 64 * G2_BN * 2, G2_STAGE = G2_A_BYTES + G2_B_BYTES;
constexpr int G2_NST = 3;
__device__ __forceinline__ int g2_swz(int k) { return (((k >> 1) & 1) << 1) | (((k >> 3) & 1) << 2); }

__global__ __launch_bounds__(256) void gemm2_kernel(
    const bf16* __restrict__ x, const bf16* __restrict__ wd, const bf16* __restrict__ dskip,
    const char* __restrict__ ws, float* __restrict__ out) {
  extern __shared__ __attribute__((aligned(16))) char smem[];
  const int tid = threadIdx.x, lane = tid & 63, wid = tid >> 6;
  const int bid = blockIdx.x;
  const int xcd = bid & 7, idx = bid >> 3;
  const int pm = xcd * 2 + (idx & 1), pn = idx >> 1;
  const int brow = pm * G2_BM, bcol = pn * G2_BN;
  const unsigned short* ah = (const unsigned short*)(ws + OFF_AH);
  const unsigned short* ct = (const unsigned short*)(ws + OFF_CT);
  const unsigned short* asrc[4];
#pragma unroll
  for (int i = 0; i < 4; ++i) {
    int b = (i * 4 + wid) * 1024 + lane * 16, R, C; a_stage_rc(b, R, C);
    asrc[i] = ah + (size_t)(brow + R) * KAH + C;
  }
  int boffs[2];
#pragma unroll
  for (int i = 0; i < 2; ++i) {
    int Lc = (i * 4 + wid) * 64 + lane, k = Lc / G2_CH, cp = Lc % G2_CH, c = cp ^ g2_swz(k);
    boffs[i] = k * DM + bcol + c * 8;
  }
  auto stage = [&](int buf, int kt) {
    char* sa = smem + buf * G2_STAGE; char* sb = sa + G2_A_BYTES;
#pragma unroll
    for (int i = 0; i < 4; ++i) GLDS16(asrc[i] + kt * 64, sa + (i * 4 + wid) * 1024);
    const unsigned short* bb = kt < 56 ? (const unsigned short*)wd + (size_t)kt * 64 * DM : ct + (size_t)(kt - 56) * 64 * DM;
#pragma unroll
    for (int i = 0; i < 2; ++i) GLDS16(bb + boffs[i], sb + (i * 4 + wid) * 1024);
  };
  const int wr = wid >> 1, wc = wid & 1;
  const int fr = lane & 15, fq = lane >> 4, tq = (lane & 15) >> 2, tp = lane & 3;
  f32x4 accd[4][2], accy[4][2];
#pragma unroll
  for (int i = 0; i < 4; ++i)
#pragma unroll
    for (int j = 0; j < 2; ++j) { accd[i][j] = (f32x4){0.f, 0.f, 0.f, 0.f}; accy[i][j] = (f32x4){0.f, 0.f, 0.f, 0.f}; }

  auto compute = [&](int buf, f32x4 (&acc)[4][2]) {
    const char* sa = smem + buf * G2_STAGE; const char* sb = sa + G2_A_BYTES;
#pragma unroll
    for (int kk = 0; kk < 2; ++kk) {
      bf16x8 af[4], bfm[2];
#pragma unroll
      for (int mi = 0; mi < 4; ++mi) af[mi] = *(const bf16x8*)(sa + a_lds_byte(wr * 64 + mi * 16 + fr, kk * 32 + fq * 8));
#pragma unroll
      for (int nj = 0; nj < 2; ++nj) {
        const int k0 = kk * 32 + 8 * fq + tq, j = wc * 2 + nj;
        const char* p0 = sb + (k0 * G2_CH + ((2 * j + (tp >> 1)) ^ g2_swz(k0))) * 16 + 8 * (tp & 1);
        const char* p1 = sb + ((k0 + 4) * G2_CH + ((2 * j + (tp >> 1)) ^ g2_swz(k0 + 4))) * 16 + 8 * (tp & 1);
        bfm[nj] = tr_frag(p0, p1);
      }
#pragma unroll
      for (int mi = 0; mi < 4; ++mi)
#pragma unroll
        for (int nj = 0; nj < 2; ++nj) acc[mi][nj] = __builtin_amdgcn_mfma_f32_16x16x32_bf16(af[mi], bfm[nj], acc[mi][nj], 0, 0, 0);
    }
  };
  stage(0, 0);
  __syncthreads();
  for (int kt = 0; kt < 56; ++kt) {
    stage((kt + 1) & 1, kt + 1);
    compute(kt & 1, accd);
    __syncthreads();
  }
  for (int kt = 56; kt < 88; ++kt) {
    if (kt + 1 < 88) stage((kt + 1) & 1, kt + 1);
    compute(kt & 1, accy);
    __syncthreads();
  }
  const unsigned short* xs = (const unsigned short*)x;
  const unsigned short* xn = (const unsigned short*)(ws + OFF_XN);
#pragma unroll
  for (int nj = 0; nj < 2; ++nj) {
    const int col = bcol + wc * 32 + nj * 16 + fr;
    const float dsk = bf2f(((const unsigned short*)dskip)[col]);
#pragma unroll
    for (int mi = 0; mi < 4; ++mi)
#pragma unroll
      for (int r = 0; r < 4; ++r) {
        const int row = brow + wr * 64 + mi * 16 + fq * 4 + r;
        const size_t o = (size_t)row * DM + col;
        const float dn = rbf(accd[mi][nj][r]);
        const float g = rbf(dn / (1.0f + expf(-dn)));
        const float s = 2.0f * accy[mi][nj][r] + rbf(bf2f(xn[o]) * dsk);
        out[o] = bf2f(xs[o]) + s * g;
      }
  }
}

extern "C" void kernel_launch(void* const* d_in, const int* in_sizes, int n_in,
                              void* d_out, int out_size, void* d_ws, size_t ws_size,
                              hipStream_t stream) {
  const bf16* x = (const bf16*)d_in[0];
  const bf16* lam_re = (const bf16*)d_in[1];
  const bf16* lam_im = (const bf16*)d_in[2];
  const bf16* b_re = (const bf16*)d_in[3];
  const bf16* b_im = (const bf16*)d_in[4];
  const bf16* c_re = (const bf16*)d_in[5];
  const bf16* c_im = (const bf16*)d_in[6];
  const bf16* log_dt = (const bf16*)d_in[7];
  const bf16* dskip = (const bf16*)d_in[8];
  const bf16* wg = (const bf16*)d_in[10];
  const bf16* wu = (const bf16*)d_in[11];
  const bf16* wd = (const bf16*)d_in[12];
  char* ws = (char*)d_ws;
  float* out = (float*)d_out;

  hipFuncSetAttribute((const void*)gemm1_kernel, hipFuncAttributeMaxDynamicSharedMemorySize, 2 * G1_STAGE);
  hipFuncSetAttribute((const void*)gemm2_kernel, hipFuncAttributeMaxDynamicSharedMemorySize, G2_NST * G2_STAGE);

  prep_kernel<<<PREP_BLOCKS, 256, 0, stream>>>(x, lam_re, lam_im, b_re, b_im, c_re, c_im, log_dt, ws);
  gemm1_kernel<<<256, 512, 2 * G1_STAGE, stream>>>(wg, wu, ws);
  scanA_kernel<<<4 * NCHUNK, 256, 0, stream>>>(ws);
  scanC_kernel<<<4 * NCHUNK, 256, 0, stream>>>(ws);
  gemm2_kernel<<<256, 256, 2 * G2_STAGE, stream>>>(x, wd, dskip, ws, out);
}
```

```cpp
#include <hip/hip_runtime.h>
#include <hip/hip_bf16.h>
#include <stdint.h>

typedef __hip_bfloat16 bf16;
typedef __attribute__((ext_vector_type(8))) short bf16x8;
typedef __attribute__((ext_vector_type(4))) short bf16x4;
typedef __attribute__((ext_vector_type(4))) float f32x4;
typedef __attribute__((__vector_size__(4 * sizeof(short)))) short v4s;

constexpr int L_SEQ = 2048, DM = 1024, FF = 3584, PP = 1024;
constexpr int LDW = 3584;
constexpr int LDXN = DM + 64;
constexpr int KAH = FF + 2 * PP + 64;
constexpr int SCAN_T = 32, NCHUNK = L_SEQ / SCAN_T;

constexpr size_t OFF_XN   = 0;
constexpr size_t OFF_BT   = OFF_XN + (size_t)L_SEQ * LDXN * 2;
constexpr size_t OFF_CT   = OFF_BT + (size_t)DM * LDW * 2;
constexpr size_t OFF_AH   = OFF_CT + (size_t)2 * PP * DM * 2;
constexpr size_t OFF_BU   = OFF_AH + (size_t)L_SEQ * KAH * 2;
constexpr size_t OFF_E    = OFF_BU + (size_t)L_SEQ * PP * 8;
constexpr size_t OFF_LAM  = OFF_E + (size_t)NCHUNK * PP * 8;
constexpr size_t OFF_COEF = OFF_LAM + (size_t)PP * 8;

__device__ __forceinline__ float bf2f(unsigned short u) { return __uint_as_float(((unsigned)u) << 16); }
__device__ __forceinline__ unsigned short f2bf(float f) {
  __bf16 b = (__bf16)f;
  return __builtin_bit_cast(unsigned short, b);
}
__device__ __forceinline__ float rbf(float f) { return bf2f(f2bf(f)); }

constexpr int PREP_LN_BLOCKS = L_SEQ / 4;
constexpr int PREP_TR_BLOCKS = 4 * (1024 / 64) * (1024 / 64);
constexpr int PREP_BLOCKS = PREP_LN_BLOCKS + PREP_TR_BLOCKS + 4;

__global__ __launch_bounds__(256) void prep_kernel(
    const bf16* __restrict__ x, const bf16* __restrict__ lam_re, const bf16* __restrict__ lam_im,
    const bf16* __restrict__ b_re, const bf16* __restrict__ b_im,
    const bf16* __restrict__ c_re, const bf16* __restrict__ c_im,
    const bf16* __restrict__ log_dt, char* __restrict__ ws) {
  __shared__ unsigned short tile[64][66];
  const int bid = blockIdx.x, tid = threadIdx.x;
  if (bid < PREP_LN_BLOCKS) {
    const int wave = tid >> 6, lane = tid & 63;
    const int row = bid * 4 + wave;
    const unsigned short* xr = (const unsigned short*)x + (size_t)row * DM;
    float v[16];
    bf16x8 a = *(const bf16x8*)(xr + lane * 8);
    bf16x8 b = *(const bf16x8*)(xr + 512 + lane * 8);
    float sum = 0.f;
#pragma unroll
    for (int j = 0; j < 8; ++j) { v[j] = bf2f((unsigned short)a[j]); v[8 + j] = bf2f((unsigned short)b[j]); }
#pragma unroll
    for (int j = 0; j < 16; ++j) sum += v[j];
#pragma unroll
    for (int o = 32; o > 0; o >>= 1) sum += __shfl_xor(sum, o);
    const float mean = sum * (1.0f / DM);
    float sq = 0.f;
#pragma unroll
    for (int j = 0; j < 16; ++j) { float d = v[j] - mean; sq += d * d; }
#pragma unroll
    for (int o = 32; o > 0; o >>= 1) sq += __shfl_xor(sq, o);
    const float var_b = rbf(sq * (1.0f / DM));
    const float mean_b = rbf(mean);
    const float den = rbf(sqrtf(rbf(var_b + 1e-5f)));
    bf16x8 oa, ob;
#pragma unroll
    for (int j = 0; j < 8; ++j) {
      oa[j] = (short)f2bf(rbf(v[j] - mean_b) / den);
      ob[j] = (short)f2bf(rbf(v[8 + j] - mean_b) / den);
    }
    unsigned short* xo = (unsigned short*)(ws + OFF_XN) + (size_t)row * LDXN;
    *(bf16x8*)(xo + lane * 8) = oa;
    *(bf16x8*)(xo + 512 + lane * 8) = ob;
  } else if (bid < PREP_LN_BLOCKS + PREP_TR_BLOCKS) {
    const int t = bid - PREP_LN_BLOCKS;
    const int mat = t >> 8, ti = (t >> 4) & 15, tj = t & 15;
    const unsigned short* src = (const unsigned short*)(mat == 0 ? b_re : mat == 1 ? b_im : mat == 2 ? c_re : c_im);
    const int tx = tid & 63, ty = tid >> 6;
#pragma unroll
    for (int r = ty; r < 64; r += 4) tile[r][tx] = src[(size_t)(ti * 64 + r) * 1024 + tj * 64 + tx];
    __syncthreads();
    unsigned short* dst; size_t ld; unsigned short neg = 0;
    if (mat < 2) { dst = (unsigned short*)(ws + OFF_BT) + mat * 1024; ld = LDW; }
    else { dst = (unsigned short*)(ws + OFF_CT) + (size_t)(mat - 2) * 1024 * DM; ld = DM; neg = (mat == 3) ? 0x8000 : 0; }
#pragma unroll
    for (int r = ty; r < 64; r += 4) dst[(size_t)(tj * 64 + r) * ld + ti * 64 + tx] = tile[tx][r] ^ neg;
  } else {
    const int p = (bid - PREP_LN_BLOCKS - PREP_TR_BLOCKS) * 256 + tid;
    const float lr = bf2f(((const unsigned short*)lam_re)[p]);
    const float li = bf2f(((const unsigned short*)lam_im)[p]);
    const float dt = rbf(expf(bf2f(((const unsigned short*)log_dt)[p])));
    const float ar = dt * lr, ai = dt * li;
    const float e = expf(ar);
    float sn, cs; sincosf(ai, &sn, &cs);
    const float br_ = e * cs, bi_ = e * sn;
    const float nr = br_ - 1.0f, ni = bi_;
    const float den = lr * lr + li * li;
    const float cr = (nr * lr + ni * li) / den, ci = (ni * lr - nr * li) / den;
    ((float2*)(ws + OFF_LAM))[p] = make_float2(br_, bi_);
    ((float2*)(ws + OFF_COEF))[p] = make_float2(cr, ci);
  }
}

__device__ __forceinline__ int a_lds_byte(int r, int c) {
  int st = (r >> 4) * 2 + (c >> 5), rr = r & 15, cc = c & 31, ob = rr * 64 + cc * 2;
  return st * 1024 + (ob ^ (((ob >> 9) & 1) << 5));
}
__device__ __forceinline__ void a_stage_rc(int b, int& R, int& C) {
  int st = b >> 10, sb = b & 1023, swz = sb ^ (((sb >> 9) & 1) << 5);
  R = (st >> 1) * 16 + (swz >> 6); C = (st & 1) * 32 + ((swz & 63) >> 1);
}
__device__ __forceinline__ bf16x8 tr_frag(const char* lds_lo, const char* lds_hi) {
  v4s lo = __builtin_amdgcn_ds_read_tr16_b64_v4i16((__attribute__((address_space(3))) v4s*)(lds_lo));
  v4s hi = __builtin_amdgcn_ds_read_tr16_b64_v4i16((__attribute__((address_space(3))) v4s*)(lds_hi));
  bf16x8 r;
  r[0] = lo[0]; r[1] = lo[1]; r[2] = lo[2]; r[3] = lo[3];
  r[4] = hi[0]; r[5] = hi[1]; r[6] = hi[2]; r[7] = hi[3];
  return r;
}
typedef __attribute__((ext_vector_type(2))) unsigned u32x2;
typedef __attribute__((ext_vector_type(4))) unsigned u32x4;
template <int OFF> __device__ __forceinline__ u32x2 lds_tr64(unsigned addr) {
  u32x2 r; asm volatile("ds_read_b64_tr_b16 %0, %1 offset:%2" : "=v"(r) : "v"(addr), "i"(OFF) : "memory"); return r;
}
template <int OFF> __device__ __forceinline__ bf16x8 lds_b128(unsigned addr) {
  u32x4 r; asm volatile("ds_read_b128 %0, %1 offset:%2" : "=v"(r) : "v"(addr), "i"(OFF) : "memory"); return __builtin_bit_cast(bf16x8, r);
}
__device__ __forceinline__ bf16x8 join_frag(u32x2 lo, u32x2 hi) {
  u32x4 r = __builtin_shufflevector(lo, hi, 0, 1, 2, 3); return __builtin_bit_cast(bf16x8, r);
}
#define WAIT_LGKM(n) do { asm volatile("s_waitcnt lgkmcnt(" #n ")" ::: "memory"); __builtin_amdgcn_sched_barrier(0); } while (0)
#define GLDS16(gptr, ldsptr) __builtin_amdgcn_global_load_lds((const void __attribute__((address_space(1)))*)(gptr), (void __attribute__((address_space(3)))*)(ldsptr), 16, 0, 0)

__device__ __forceinline__ float gelu_tanh_f(float x) {
  const float k0 = 0.7978845608028654f, k1 = 0.044715f;
  float u = k0 * (x + k1 * x * x * x);
  return x * __builtin_amdgcn_rcpf(1.0f + __expf(-2.0f * u));
}

constexpr int G1_BM = 256, G1_BN = 288, G1_CH = G1_BN / 8;
constexpr int G1_BK = 32, G1_NST = 4, G1_NK = 1024 / G1_BK;
constexpr int G1_A_BYTES = G1_BM * G1_BK * 2, G1_B_BYTES = G1_BK * G1_BN * 2, G1_STAGE = G1_A_BYTES + G1_B_BYTES;
__device__ __forceinline__ int g1_swz(int k) { return ((k >> 3) & 1) << 1; }
#define WAIT_VM(n) asm volatile("s_waitcnt vmcnt(" #n ") lgkmcnt(0)" ::: "memory")
#define RAW_BAR() __builtin_amdgcn_s_barrier()

__global__ __launch_bounds__(512, 2) void gemm1_kernel(
    const bf16* __restrict__ wg, const bf16* __restrict__ wu, char* __restrict__ ws) {
  extern __shared__ __attribute__((aligned(16))) char smem[];
  const int tid = threadIdx.x, lane = tid & 63, wid = __builtin_amdgcn_readfirstlane(tid >> 6);
  const int bid = blockIdx.x;
  const int xcd = bid & 7, idx = bid >> 3;
  const int pn = xcd * 4 + (idx & 3), pm = idx >> 2;
  const int brow = pm * G1_BM;
  const unsigned short* xn = (const unsigned short*)(ws + OFF_XN);
  const unsigned short* bt = (const unsigned short*)(ws + OFF_BT);

  unsigned aoffs[2];
#pragma unroll
  for (int i = 0; i < 2; ++i) {
    int sb = lane * 16, swz = sb ^ (((sb >> 9) & 1) << 5);
    int R = (i * 8 + wid) * 16 + (swz >> 6), C = (swz & 63) >> 1;
    aoffs[i] = (unsigned)((brow + R) * LDXN + C);
  }
  const unsigned short* bsrc[3];
#pragma unroll
  for (int i = 0; i < 3; ++i) {
    int inst = i * 8 + wid; if (inst > 17) inst = 17;
    int Lc = inst * 64 + lane, k = Lc / G1_CH, cp = Lc % G1_CH, c = cp ^ g1_swz(k);
    int half = c >= 18, cc = c - half * 18, grp = pn * 9 + (cc >> 1), col = (cc & 1) * 8;
    const unsigned short* base = (const unsigned short*)(half ? wu : wg);
    int o = grp * 16;
    if (grp >= 224) { base = bt; o = half * 1024 + (grp - 224) * 16; }
    bsrc[i] = base + (k * LDW + col + o);
  }
#define G1_STAGE_LOADS(buf) do { \
    char* sa_ = smem + (buf) * G1_STAGE; char* sb_ = sa_ + G1_A_BYTES; \
    _Pragma("unroll") for (int i = 0; i < 2; ++i) { GLDS16(xn + aoffs[i], sa_ + (i * 8 + wid) * 1024); aoffs[i] += G1_BK; } \
    _Pragma("unroll") for (int i = 0; i < 2; ++i) { GLDS16(bsrc[i], sb_ + (i * 8 + wid) * 1024); bsrc[i] += G1_BK * LDW; } \
    if (wid < 2) { GLDS16(bsrc[2], sb_ + (16 + wid) * 1024); bsrc[2] += G1_BK * LDW; } \
  } while (0)
  f32x4 acc[2][18];
#pragma unroll
  for (int i = 0; i < 2; ++i)
#pragma unroll
    for (int j = 0; j < 18; ++j) acc[i][j] = (f32x4){0.f, 0.f, 0.f, 0.f};

  const int fr = lane & 15, fq = lane >> 4, tq = (lane & 15) >> 2, tp = lane & 3;
  const int ob_ = fr * 64 + fq * 16, aoff0 = (wid * 2) * 1024 + (ob_ ^ (((ob_ >> 9) & 1) << 5));
  const int bRow = G1_A_BYTES + ((8 * fq + tq) * G1_CH + (tp >> 1)) * 16 + 8 * (tp & 1);
  const int bEven = bRow + 32 * (fq & 1), bOdd = bRow - 32 * (fq & 1);

  const unsigned ldsb = (unsigned)(size_t)(__attribute__((address_space(3))) char*)smem;
  const unsigned aA = ldsb + aoff0, aE = ldsb + bEven, aO = ldsb + bOdd;
#define G1_RDB(dst, buf, jg) do { \
    dst[0] = join_frag(lds_tr64<32 * ((jg) * 6 + 0)>(aE + (buf) * G1_STAGE), lds_tr64<32 * ((jg) * 6 + 0) + 4 * G1_CH * 16>(aE + (buf) * G1_STAGE)); \
    dst[1] = join_frag(lds_tr64<32 * ((jg) * 6 + 1)>(aO + (buf) * G1_STAGE), lds_tr64<32 * ((jg) * 6 + 1) + 4 * G1_CH * 16>(aO + (buf) * G1_STAGE)); \
    dst[2] = join_frag(lds_tr64<32 * ((jg) * 6 + 2)>(aE + (buf) * G1_STAGE), lds_tr64<32 * ((jg) * 6 + 2) + 4 * G1_CH * 16>(aE + (buf) * G1_STAGE)); \
    dst[3] = join_frag(lds_tr64<32 * ((jg) * 6 + 3)>(aO + (buf) * G1_STAGE), lds_tr64<32 * ((jg) * 6 + 3) + 4 * G1_CH * 16>(aO + (buf) * G1_STAGE)); \
    dst[4] = join_frag(lds_tr64<32 * ((jg) * 6 + 4)>(aE + (buf) * G1_STAGE), lds_tr64<32 * ((jg) * 6 + 4) + 4 * G1_CH * 16>(aE + (buf) * G1_STAGE)); \
    dst[5] = join_frag(lds_tr64<32 * ((jg) * 6 + 5)>(aO + (buf) * G1_STAGE), lds_tr64<32 * ((jg) * 6 + 5) + 4 * G1_CH * 16>(aO + (buf) * G1_STAGE)); \
  } while (0)
#define G1_MMA(src, jg) do { \
    _Pragma("unroll") for (int jj = 0; jj < 6; ++jj) \
      _Pragma("unroll") for (int mi = 0; mi < 2; ++mi) \
        acc[mi][(jg) * 6 + jj] = __builtin_amdgcn_mfma_f32_16x16x32_bf16(af[mi], src[jj], acc[mi][(jg) * 6 + jj], 0, 0, 0); \
  } while (0)
#define G1_COMPUTE(buf) do { \
    bf16x8 af[2], b0[6], b1[6]; \
    af[0] = lds_b128<0>(aA + (buf) * G1_STAGE); af[1] = lds_b128<1024>(aA + (buf) * G1_STAGE); \
    G1_RDB(b0, buf, 0); \
    WAIT_LGKM(0); \
    G1_RDB(b1, buf, 1); \
    G1_MMA(b0, 0); \
    WAIT_LGKM(0); \
    G1_RDB(b0, buf, 2); \
    G1_MMA(b1, 1); \
    WAIT_LGKM(0); \
    G1_MMA(b0, 2); \
  } while (0)

  G1_STAGE_LOADS(0); G1_STAGE_LOADS(1); G1_STAGE_LOADS(2);
#define G1_STEP(u) do { if (wid < 2) WAIT_VM(10); else WAIT_VM(8); RAW_BAR(); G1_STAGE_LOADS(((u) + 3) & 3); G1_COMPUTE(u); } while (0)
  for (int kt0 = 0; kt0 < G1_NK - 4; kt0 += 4) {
    G1_STEP(0); G1_STEP(1); G1_STEP(2); G1_STEP(3);
  }
  if (wid < 2) WAIT_VM(10); else WAIT_VM(8);
  RAW_BAR(); G1_STAGE_LOADS(3); G1_COMPUTE(0);
  if (wid < 2) WAIT_VM(10); else WAIT_VM(8);
  RAW_BAR(); G1_COMPUTE(1);
  if (wid < 2) WAIT_VM(5); else WAIT_VM(4);
  RAW_BAR(); G1_COMPUTE(2);
  WAIT_VM(0);
  RAW_BAR(); G1_COMPUTE(3);
  int tz = threadIdx.x; asm volatile("" : "+v"(tz));
  const int lane2 = tz & 63, fr2 = tz & 15, fq2 = (tz >> 4) & 3;
  unsigned short* ah = (unsigned short*)(ws + OFF_AH);
  float2* bu = (float2*)(ws + OFF_BU);
  const float2* coef = (const float2*)(ws + OFF_COEF);
  const int nf = min(9, max(0, 224 - pn * 9));
  WAIT_VM(0); RAW_BAR();
  constexpr int EP_LD = 304;
  char* wl = smem + wid * (32 * EP_LD);
#pragma unroll
  for (int j = 0; j < 9; ++j) {
    if (j < nf) {
#pragma unroll
      for (int mi = 0; mi < 2; ++mi)
#pragma unroll
        for (int r = 0; r < 4; ++r) {
          float g = rbf(acc[mi][j][r]), u = rbf(acc[mi][9 + j][r]);
          float a = rbf(gelu_tanh_f(g)) * u;
          *(unsigned short*)(wl + (mi * 16 + fq2 * 4 + r) * EP_LD + (j * 16 + fr2) * 2) = f2bf(a);
        }
    } else {
      const int p = (pn * 9 + j - 224) * 16 + fr2;
      const float2 cf = coef[p];
#pragma unroll
      for (int mi = 0; mi < 2; ++mi)
#pragma unroll
        for (int r = 0; r < 4; ++r) {
          const int row = brow + wid * 32 + mi * 16 + fq2 * 4 + r;
          float ur = acc[mi][j][r], ui = acc[mi][9 + j][r];
          bu[(size_t)row * PP + p] = make_float2(cf.x * ur - cf.y * ui, cf.x * ui + cf.y * ur);
        }
    }
  }
  asm volatile("s_waitcnt lgkmcnt(0)" ::: "memory");
  {
    const int nch = nf * 2;
    unsigned short* dst0 = ah + (size_t)(brow + wid * 32) * KAH + pn * 144;
#pragma unroll
    for (int i = 0; i < 9; ++i) {
      const int q = i * 64 + lane2, row = q / 18, ch = q % 18;
      if (ch < nch) {
        bf16x8 v = *(const bf16x8*)(wl + row * EP_LD + ch * 16);
        *(bf16x8*)(dst0 + (size_t)row * KAH + ch * 8) = v;
      }
    }
  }
}

__global__ __launch_bounds__(256) void scanA_kernel(char* __restrict__ ws) {
  const int p = (blockIdx.x & 3) * 256 + threadIdx.x, c = blockIdx.x >> 2;
  const float2 lam = ((const float2*)(ws + OFF_LAM))[p];
  const float2* bu = (const float2*)(ws + OFF_BU) + (size_t)c * SCAN_T * PP + p;
  float er = 0.f, ei = 0.f;
#pragma unroll 8
  for (int s = 0; s < SCAN_T; ++s) {
    float2 b = bu[(size_t)s * PP];
    float nr = lam.x * er - lam.y * ei + b.x, ni = lam.x * ei + lam.y * er + b.y;
    er = nr; ei = ni;
  }
  ((float2*)(ws + OFF_E))[(size_t)c * PP + p] = make_float2(er, ei);
}

__global__ __launch_bounds__(256) void scanC_kernel(char* __restrict__ ws) {
  const int p = (blockIdx.x & 3) * 256 + threadIdx.x, c = blockIdx.x >> 2;
  const float2 lam = ((const float2*)(ws + OFF_LAM))[p];
  float lr = lam.x, li = lam.y;
#pragma unroll
  for (int i = 0; i < 5; ++i) { float a = lr * lr - li * li, b = 2.f * lr * li; lr = a; li = b; }
  const float2* E = (const float2*)(ws + OFF_E) + p;
  float hr = 0.f, hi = 0.f;
  int cc = 0;
  for (; cc + 8 <= c; cc += 8) {
    float2 e[8];
#pragma unroll
    for (int u = 0; u < 8; ++u) e[u] = E[(size_t)(cc + u) * PP];
#pragma unroll
    for (int u = 0; u < 8; ++u) { float nr = lr * hr - li * hi + e[u].x, ni = lr * hi + li * hr + e[u].y; hr = nr; hi = ni; }
  }
  for (; cc < c; ++cc) {
    float2 e = E[(size_t)cc * PP];
    float nr = lr * hr - li * hi + e.x, ni = lr * hi + li * hr + e.y;
    hr = nr; hi = ni;
  }
  const float2* bu = (const float2*)(ws + OFF_BU) + (size_t)c * SCAN_T * PP + p;
  unsigned short* ah = (unsigned short*)(ws + OFF_AH) + (size_t)c * SCAN_T * KAH + FF + p;
#pragma unroll 8
  for (int s = 0; s < SCAN_T; ++s) {
    float2 b = bu[(size_t)s * PP];
    float nr = lam.x * hr - lam.y * hi + b.x, ni = lam.x * hi + lam.y * hr + b.y;
    hr = nr; hi = ni;
    ah[(size_t)s * KAH] = f2bf(hr);
    ah[(size_t)s * KAH + PP] = f2bf(hi);
  }
}

constexpr int G2_BM = 128, G2_BN = 64, G2_CH = G2_BN / 8;
constexpr int G2_A_BYTES = G2_BM * 64 * 2, G2_B_BYTES = 64 * G2_BN * 2, G2_STAGE = G2_A_BYTES + G2_B_BYTES;
constexpr int G2_NST = 5;
__device__ __forceinline__ int g2_swz(int k) { return (((k >> 1) & 1) << 1) | (((k >> 3) & 1) << 2); }

__global__ __launch_bounds__(256) void gemm2_kernel(
    const bf16* __restrict__ x, const bf16* __restrict__ wd, const bf16* __restrict__ dskip,
    const char* __restrict__ ws, float* __restrict__ out) {
  extern __shared__ __attribute__((aligned(16))) char smem[];
  const int tid = threadIdx.x, lane = tid & 63, wid = __builtin_amdgcn_readfirstlane(tid >> 6);
  const int bid = blockIdx.x;
  const int xcd = bid & 7, idx = bid >> 3;
  const int pm = xcd * 2 + (idx & 1), pn = idx >> 1;
  const int brow = pm * G2_BM, bcol = pn * G2_BN;
  const unsigned short* ah = (const unsigned short*)(ws + OFF_AH);
  const unsigned short* ct = (const unsigned short*)(ws + OFF_CT);
  const unsigned short* asrc[4];
#pragma unroll
  for (int i = 0; i < 4; ++i) {
    int R = (i * 4 + wid) * 8 + (lane >> 3), C = ((lane & 7) ^ (R & 7)) * 8;
    asrc[i] = ah + (size_t)(brow + R) * KAH + C;
  }
  int boffs[2];
#pragma unroll
  for (int i = 0; i < 2; ++i) {
    int Lc = (i * 4 + wid) * 64 + lane, k = Lc / G2_CH, cp = Lc % G2_CH, c = cp ^ g2_swz(k);
    boffs[i] = k * DM + bcol + c * 8;
  }
#define ABL_A(x) x
#define ABL_B(x) x
#define G2_STAGE_LOADS(buf, kt) do { \
    char* sa_ = smem + (buf) * G2_STAGE; char* sb_ = sa_ + G2_A_BYTES; \
    ABL_A(_Pragma("unroll") for (int i = 0; i < 4; ++i) GLDS16(asrc[i] + (kt) * 64, sa_ + (i * 4 + wid) * 1024);) \
    const unsigned short* bb_ = (kt) < 56 ? (const unsigned short*)wd + (size_t)(kt) * 64 * DM : ct + (size_t)((kt) - 56) * 64 * DM; \
    ABL_B(_Pragma("unroll") for (int i = 0; i < 2; ++i) GLDS16(bb_ + boffs[i], sb_ + (i * 4 + wid) * 1024);) \
  } while (0)
  const int wr = wid >> 1, wc = wid & 1;
  const int fr = lane & 15, fq = lane >> 4, tq = (lane & 15) >> 2, tp = lane & 3;
  f32x4 accd[4][2], accy[4][2];
#pragma unroll
  for (int i = 0; i < 4; ++i)
#pragma unroll
    for (int j = 0; j < 2; ++j) { accd[i][j] = (f32x4){0.f, 0.f, 0.f, 0.f}; accy[i][j] = (f32x4){0.f, 0.f, 0.f, 0.f}; }

  const unsigned ldsb = (unsigned)(size_t)(__attribute__((address_space(3))) char*)smem;
  const unsigned aA0 = ldsb + (wr * 64 + fr) * 128 + ((fq ^ (fr & 7)) * 16);
  const unsigned aA1 = ldsb + (wr * 64 + fr) * 128 + (((4 + fq) ^ (fr & 7)) * 16);
  unsigned aB[2][2];
#pragma unroll
  for (int nj = 0; nj < 2; ++nj)
#pragma unroll
    for (int t = 0; t < 2; ++t) {
      const int k0 = 8 * fq + tq + 4 * t, j = wc * 2 + nj;
      aB[nj][t] = ldsb + G2_A_BYTES + (k0 * G2_CH + ((2 * j + (tp >> 1)) ^ g2_swz(k0))) * 16 + 8 * (tp & 1);
    }
#define G2_RD(af, bfm, buf, kk) do { \
    bfm[0] = join_frag(lds_tr64<(kk) * 32 * G2_CH * 16>(aB[0][0] + (buf) * G2_STAGE), lds_tr64<(kk) * 32 * G2_CH * 16>(aB[0][1] + (buf) * G2_STAGE)); \
    bfm[1] = join_frag(lds_tr64<(kk) * 32 * G2_CH * 16>(aB[1][0] + (buf) * G2_STAGE), lds_tr64<(kk) * 32 * G2_CH * 16>(aB[1][1] + (buf) * G2_STAGE)); \
    af[0] = lds_b128<0>(((kk) ? aA1 : aA0) + (buf) * G2_STAGE); af[1] = lds_b128<2048>(((kk) ? aA1 : aA0) + (buf) * G2_STAGE); \
    af[2] = lds_b128<4096>(((kk) ? aA1 : aA0) + (buf) * G2_STAGE); af[3] = lds_b128<6144>(((kk) ? aA1 : aA0) + (buf) * G2_STAGE); \
  } while (0)
#define G2_MMA(acc, af, bfm) do { \
    _Pragma("unroll") for (int mi = 0; mi < 4; ++mi) \
      _Pragma("unroll") for (int nj = 0; nj < 2; ++nj) acc[mi][nj] = __builtin_amdgcn_mfma_f32_16x16x32_bf16(af[mi], bfm[nj], acc[mi][nj], 0, 0, 0); \
  } while (0)
#define compute(buf, acc) do { \
    bf16x8 a0[4], a1[4], f0[2], f1[2]; \
    G2_RD(a0, f0, buf, 0); \
    WAIT_LGKM(0); \
    G2_RD(a1, f1, buf, 1); \
    G2_MMA(acc, a0, f0); \
    WAIT_LGKM(0); \
    G2_MMA(acc, a1, f1); \
  } while (0)
  G2_STAGE_LOADS(0, 0); G2_STAGE_LOADS(1, 1); G2_STAGE_LOADS(2, 2); G2_STAGE_LOADS(3, 3);
#define G2_STEP(u, kt, acc) do { WAIT_VM(18); RAW_BAR(); G2_STAGE_LOADS(((u) + 4) % 5, (kt) + 4); compute(u, acc); } while (0)
  for (int kt0 = 0; kt0 < 55; kt0 += 5) {
    G2_STEP(0, kt0, accd); G2_STEP(1, kt0 + 1, accd); G2_STEP(2, kt0 + 2, accd); G2_STEP(3, kt0 + 3, accd); G2_STEP(4, kt0 + 4, accd);
  }
  WAIT_VM(18); RAW_BAR(); G2_STAGE_LOADS(4, 59); compute(0, accd);
  for (int kt0 = 56; kt0 < 81; kt0 += 5) {
    G2_STEP(1, kt0, accy); G2_STEP(2, kt0 + 1, accy); G2_STEP(3, kt0 + 2, accy); G2_STEP(4, kt0 + 3, accy); G2_STEP(0, kt0 + 4, accy);
  }
  WAIT_VM(18); RAW_BAR(); G2_STAGE_LOADS(0, 85); compute(1, accy);
  WAIT_VM(18); RAW_BAR(); G2_STAGE_LOADS(1, 86); compute(2, accy);
  WAIT_VM(18); RAW_BAR(); G2_STAGE_LOADS(2, 87); compute(3, accy);
  WAIT_VM(18); RAW_BAR(); compute(4, accy);
  WAIT_VM(12); RAW_BAR(); compute(0, accy);
  WAIT_VM(6); RAW_BAR(); compute(1, accy);
  WAIT_VM(0); RAW_BAR(); compute(2, accy);
  const unsigned short* xs = (const unsigned short*)x;
  const unsigned short* xn = (const unsigned short*)(ws + OFF_XN);
  WAIT_VM(0); RAW_BAR();
  constexpr int EP_LD = 32 * 8 + 16;
  char* wl = smem + wid * (64 * EP_LD);
#pragma unroll
  for (int nj = 0; nj < 2; ++nj)
#pragma unroll
    for (int mi = 0; mi < 4; ++mi)
#pragma unroll
      for (int r = 0; r < 4; ++r) {
        const float dn = rbf(accd[mi][nj][r]);
        const float g = rbf(dn * __builtin_amdgcn_rcpf(1.0f + __expf(-dn)));
        *(float2*)(wl + (mi * 16 + fq * 4 + r) * EP_LD + (nj * 16 + fr) * 8) = make_float2(2.0f * accy[mi][nj][r], g);
      }
  asm volatile("s_waitcnt lgkmcnt(0)" ::: "memory");
  {
    const int c4 = (lane & 7) * 4, rsub = lane >> 3;
    const int col = bcol + wc * 32 + c4;
    const unsigned short* dk = (const unsigned short*)dskip + col;
    const bf16x4 dk4 = *(const bf16x4*)dk;
#pragma unroll
    for (int i = 0; i < 8; ++i) {
      const int rl = i * 8 + rsub, row = brow + wr * 64 + rl;
      const size_t o = (size_t)row * DM + col;
      const bf16x4 x4 = *(const bf16x4*)(xs + o), n4 = *(const bf16x4*)(xn + (size_t)row * LDXN + col);
      const f32x4 p0 = *(const f32x4*)(wl + rl * EP_LD + c4 * 8), p1 = *(const f32x4*)(wl + rl * EP_LD + c4 * 8 + 16);
      f32x4 res;
      res[0] = bf2f((unsigned short)x4[0]) + (p0[0] + rbf(bf2f((unsigned short)n4[0]) * bf2f((unsigned short)dk4[0]))) * p0[1];
      res[1] = bf2f((unsigned short)x4[1]) + (p0[2] + rbf(bf2f((unsigned short)n4[1]) * bf2f((unsigned short)dk4[1]))) * p0[3];
      res[2] = bf2f((unsigned short)x4[2]) + (p1[0] + rbf(bf2f((unsigned short)n4[2]) * bf2f((unsigned short)dk4[2]))) * p1[1];
      res[3] = bf2f((unsigned short)x4[3]) + (p1[2] + rbf(bf2f((unsigned short)n4[3]) * bf2f((unsigned short)dk4[3]))) * p1[3];
      *(f32x4*)(out + o) = res;
    }
  }
}

extern "C" void kernel_launch(void* const* d_in, const int* in_sizes, int n_in,
                              void* d_out, int out_size, void* d_ws, size_t ws_size,
                              hipStream_t stream) {
  const bf16* x = (const bf16*)d_in[0];
  const bf16* lam_re = (const bf16*)d_in[1];
  const bf16* lam_im = (const bf16*)d_in[2];
  const bf16* b_re = (const bf16*)d_in[3];
  const bf16* b_im = (const bf16*)d_in[4];
  const bf16* c_re = (const bf16*)d_in[5];
  const bf16* c_im = (const bf16*)d_in[6];
  const bf16* log_dt = (const bf16*)d_in[7];
  const bf16* dskip = (const bf16*)d_in[8];
  const bf16* wg = (const bf16*)d_in[10];
  const bf16* wu = (const bf16*)d_in[11];
  const bf16* wd = (const bf16*)d_in[12];
  char* ws = (char*)d_ws;
  float* out = (float*)d_out;

  hipFuncSetAttribute((const void*)gemm1_kernel, hipFuncAttributeMaxDynamicSharedMemorySize, G1_NST * G1_STAGE);
  hipFuncSetAttribute((const void*)gemm2_kernel, hipFuncAttributeMaxDynamicSharedMemorySize, G2_NST * G2_STAGE);

  prep_kernel<<<PREP_BLOCKS, 256, 0, stream>>>(x, lam_re, lam_im, b_re, b_im, c_re, c_im, log_dt, ws);
  gemm1_kernel<<<256, 512, G1_NST * G1_STAGE, stream>>>(wg, wu, ws);
  scanA_kernel<<<4 * NCHUNK, 256, 0, stream>>>(ws);
  scanC_kernel<<<4 * NCHUNK, 256, 0, stream>>>(ws);
  gemm2_kernel<<<256, 256, G2_NST * G2_STAGE, stream>>>(x, wd, dskip, ws, out);
}
```

```cpp
#include <hip/hip_runtime.h>
#include <hip/hip_bf16.h>
#include <stdint.h>

typedef __hip_bfloat16 bf16;
typedef __attribute__((ext_vector_type(8))) short bf16x8;
typedef __attribute__((ext_vector_type(4))) short bf16x4;
typedef __attribute__((ext_vector_type(4))) float f32x4;
typedef __attribute__((__vector_size__(4 * sizeof(short)))) short v4s;

constexpr int L_SEQ = 2048, DM = 1024, FF = 3584, PP = 1024;
constexpr int LDW = 3584;
constexpr int LDXN = DM + 64;
constexpr int KAH = FF + 2 * PP + 64;
constexpr int SCAN_T = 32, NCHUNK = L_SEQ / SCAN_T;

constexpr size_t OFF_XN   = 0;
constexpr size_t OFF_BT   = OFF_XN + (size_t)L_SEQ * LDXN * 2;
constexpr size_t OFF_CT   = OFF_BT + (size_t)DM * LDW * 2;
constexpr size_t OFF_AH   = OFF_CT + (size_t)2 * PP * DM * 2;
constexpr size_t OFF_BU   = OFF_AH + (size_t)L_SEQ * KAH * 2;
constexpr size_t OFF_E    = OFF_BU + (size_t)L_SEQ * PP * 8;
constexpr size_t OFF_LAM  = OFF_E + (size_t)NCHUNK * PP * 8;
constexpr size_t OFF_COEF = OFF_LAM + (size_t)PP * 8;
constexpr size_t OFF_CNT  = OFF_COEF + (size_t)PP * 8;
constexpr size_t OFF_PART = OFF_CNT + 4096;

__device__ __forceinline__ float bf2f(unsigned short u) { return __uint_as_float(((unsigned)u) << 16); }
__device__ __forceinline__ unsigned short f2bf(float f) {
  __bf16 b = (__bf16)f;
  return __builtin_bit_cast(unsigned short, b);
}
__device__ __forceinline__ float rbf(float f) { return bf2f(f2bf(f)); }

constexpr int PREP_LN_BLOCKS = L_SEQ / 4;
constexpr int PREP_TR_BLOCKS = 4 * (1024 / 64) * (1024 / 64);
constexpr int PREP_BLOCKS = PREP_LN_BLOCKS + PREP_TR_BLOCKS + 4;

__global__ __launch_bounds__(256) void prep_kernel(
    const bf16* __restrict__ x, const bf16* __restrict__ lam_re, const bf16* __restrict__ lam_im,
    const bf16* __restrict__ b_re, const bf16* __restrict__ b_im,
    const bf16* __restrict__ c_re, const bf16* __restrict__ c_im,
    const bf16* __restrict__ log_dt, char* __restrict__ ws) {
  __shared__ unsigned short tile[64][66];
  const int bid = blockIdx.x, tid = threadIdx.x;
  if (bid < PREP_LN_BLOCKS) {
    const int wave = tid >> 6, lane = tid & 63;
    const int row = bid * 4 + wave;
    const unsigned short* xr = (const unsigned short*)x + (size_t)row * DM;
    float v[16];
    bf16x8 a = *(const bf16x8*)(xr + lane * 8);
    bf16x8 b = *(const bf16x8*)(xr + 512 + lane * 8);
    float sum = 0.f;
#pragma unroll
    for (int j = 0; j < 8; ++j) { v[j] = bf2f((unsigned short)a[j]); v[8 + j] = bf2f((unsigned short)b[j]); }
#pragma unroll
    for (int j = 0; j < 16; ++j) sum += v[j];
#pragma unroll
    for (int o = 32; o > 0; o >>= 1) sum += __shfl_xor(sum, o);
    const float mean = sum * (1.0f / DM);
    float sq = 0.f;
#pragma unroll
    for (int j = 0; j < 16; ++j) { float d = v[j] - mean; sq += d * d; }
#pragma unroll
    for (int o = 32; o > 0; o >>= 1) sq += __shfl_xor(sq, o);
    const float var_b = rbf(sq * (1.0f / DM));
    const float mean_b = rbf(mean);
    const float den = rbf(sqrtf(rbf(var_b + 1e-5f)));
    bf16x8 oa, ob;
#pragma unroll
    for (int j = 0; j < 8; ++j) {
      oa[j] = (short)f2bf(rbf(v[j] - mean_b) / den);
      ob[j] = (short)f2bf(rbf(v[8 + j] - mean_b) / den);
    }
    unsigned short* xo = (unsigned short*)(ws + OFF_XN) + (size_t)row * LDXN;
    *(bf16x8*)(xo + lane * 8) = oa;
    *(bf16x8*)(xo + 512 + lane * 8) = ob;
  } else if (bid < PREP_LN_BLOCKS + PREP_TR_BLOCKS) {
    const int t = bid - PREP_LN_BLOCKS;
    const int mat = t >> 8, ti = (t >> 4) & 15, tj = t & 15;
    const unsigned short* src = (const unsigned short*)(mat == 0 ? b_re : mat == 1 ? b_im : mat == 2 ? c_re : c_im);
    const int tc = tid & 7, tr = tid >> 3;
#pragma unroll
    for (int i = 0; i < 2; ++i) {
      const int r = tr + 32 * i;
      bf16x8 v = *(const bf16x8*)(src + (size_t)(ti * 64 + r) * 1024 + tj * 64 + tc * 8);
#pragma unroll
      for (int e = 0; e < 8; ++e) tile[r][tc * 8 + e] = (unsigned short)v[e];
    }
    __syncthreads();
    unsigned short* dst; size_t ld; unsigned short neg = 0;
    if (mat < 2) { dst = (unsigned short*)(ws + OFF_BT) + mat * 1024; ld = LDW; }
    else { dst = (unsigned short*)(ws + OFF_CT) + (size_t)(mat - 2) * 1024 * DM; ld = DM; neg = (mat == 3) ? 0x8000 : 0; }
#pragma unroll
    for (int i = 0; i < 2; ++i) {
      const int c = tr + 32 * i;
      bf16x8 v;
#pragma unroll
      for (int e = 0; e < 8; ++e) v[e] = (short)(tile[tc * 8 + e][c] ^ neg);
      *(bf16x8*)(dst + (size_t)(tj * 64 + c) * ld + ti * 64 + tc * 8) = v;
    }
  } else {
    const int p = (bid - PREP_LN_BLOCKS - PREP_TR_BLOCKS) * 256 + tid;
    if (p < 64) ((unsigned*)(ws + OFF_CNT))[p] = 0u;
    const float lr = bf2f(((const unsigned short*)lam_re)[p]);
    const float li = bf2f(((const unsigned short*)lam_im)[p]);
    const float dt = rbf(expf(bf2f(((const unsigned short*)log_dt)[p])));
    const float ar = dt * lr, ai = dt * li;
    const float e = expf(ar);
    float sn, cs; sincosf(ai, &sn, &cs);
    const float br_ = e * cs, bi_ = e * sn;
    const float nr = br_ - 1.0f, ni = bi_;
    const float den = lr * lr + li * li;
    const float cr = (nr * lr + ni * li) / den, ci = (ni * lr - nr * li) / den;
    ((float2*)(ws + OFF_LAM))[p] = make_float2(br_, bi_);
    ((float2*)(ws + OFF_COEF))[p] = make_float2(cr, ci);
  }
}

__device__ __forceinline__ int a_lds_byte(int r, int c) {
  int st = (r >> 4) * 2 + (c >> 5), rr = r & 15, cc = c & 31, ob = rr * 64 + cc * 2;
  return st * 1024 + (ob ^ (((ob >> 9) & 1) << 5));
}
__device__ __forceinline__ void a_stage_rc(int b, int& R, int& C) {
  int st = b >> 10, sb = b & 1023, swz = sb ^ (((sb >> 9) & 1) << 5);
  R = (st >> 1) * 16 + (swz >> 6); C = (st & 1) * 32 + ((swz & 63) >> 1);
}
__device__ __forceinline__ bf16x8 tr_frag(const char* lds_lo, const char* lds_hi) {
  v4s lo = __builtin_amdgcn_ds_read_tr16_b64_v4i16((__attribute__((address_space(3))) v4s*)(lds_lo));
  v4s hi = __builtin_amdgcn_ds_read_tr16_b64_v4i16((__attribute__((address_space(3))) v4s*)(lds_hi));
  bf16x8 r;
  r[0] = lo[0]; r[1] = lo[1]; r[2] = lo[2]; r[3] = lo[3];
  r[4] = hi[0]; r[5] = hi[1]; r[6] = hi[2]; r[7] = hi[3];
  return r;
}
typedef __attribute__((ext_vector_type(2))) unsigned u32x2;
typedef __attribute__((ext_vector_type(4))) unsigned u32x4;
template <int OFF> __device__ __forceinline__ u32x2 lds_tr64(unsigned addr) {
  u32x2 r; asm volatile("ds_read_b64_tr_b16 %0, %1 offset:%2" : "=v"(r) : "v"(addr), "i"(OFF) : "memory"); return r;
}
template <int OFF> __device__ __forceinline__ bf16x8 lds_b128(unsigned addr) {
  u32x4 r; asm volatile("ds_read_b128 %0, %1 offset:%2" : "=v"(r) : "v"(addr), "i"(OFF) : "memory"); return __builtin_bit_cast(bf16x8, r);
}
__device__ __forceinline__ bf16x8 join_frag(u32x2 lo, u32x2 hi) {
  u32x4 r = __builtin_shufflevector(lo, hi, 0, 1, 2, 3); return __builtin_bit_cast(bf16x8, r);
}
#define WAIT_LGKM(n) do { asm volatile("s_waitcnt lgkmcnt(" #n ")" ::: "memory"); __builtin_amdgcn_sched_barrier(0); } while (0)
#define GLDS16(gptr, ldsptr) __builtin_amdgcn_global_load_lds((const void __attribute__((address_space(1)))*)(gptr), (void __attribute__((address_space(3)))*)(ldsptr), 16, 0, 0)

__device__ __forceinline__ float gelu_tanh_f(float x) {
  const float k0 = 0.7978845608028654f, k1 = 0.044715f;
  float u = k0 * (x + k1 * x * x * x);
  return x * __builtin_amdgcn_rcpf(1.0f + __expf(-2.0f * u));
}

constexpr int G1_BM = 256, G1_BN = 288, G1_CH = G1_BN / 8;
constexpr int G1_BK = 32, G1_NST = 4, G1_NK = 1024 / G1_BK;
constexpr int G1_A_BYTES = G1_BM * G1_BK * 2, G1_B_BYTES = G1_BK * G1_BN * 2, G1_STAGE = G1_A_BYTES + G1_B_BYTES;
__device__ __forceinline__ int g1_swz(int k) { return ((k >> 3) & 1) << 1; }
#define WAIT_VM(n) asm volatile("s_waitcnt vmcnt(" #n ") lgkmcnt(0)" ::: "memory")
#define RAW_BAR() __builtin_amdgcn_s_barrier()

__global__ __launch_bounds__(512, 2) void gemm1_kernel(
    const bf16* __restrict__ wg, const bf16* __restrict__ wu, char* __restrict__ ws) {
  extern __shared__ __attribute__((aligned(16))) char smem[];
  const int tid = threadIdx.x, lane = tid & 63, wid = __builtin_amdgcn_readfirstlane(tid >> 6);
  const int bid = blockIdx.x;
  const int xcd = bid & 7, idx = bid >> 3;
  const int pn = xcd * 4 + (idx & 3), pm = idx >> 2;
  const int brow = pm * G1_BM;
  const unsigned short* xn = (const unsigned short*)(ws + OFF_XN);
  const unsigned short* bt = (const unsigned short*)(ws + OFF_BT);

  unsigned aoffs[2];
#pragma unroll
  for (int i = 0; i < 2; ++i) {
    int sb = lane * 16, swz = sb ^ (((sb >> 9) & 1) << 5);
    int R = (i * 8 + wid) * 16 + (swz >> 6), C = (swz & 63) >> 1;
    aoffs[i] = (unsigned)((brow + R) * LDXN + C);
  }
  const unsigned short* bsrc[3];
#pragma unroll
  for (int i = 0; i < 3; ++i) {
    int inst = i * 8 + wid; if (inst > 17) inst = 17;
    int Lc = inst * 64 + lane, k = Lc / G1_CH, cp = Lc % G1_CH, c = cp ^ g1_swz(k);
    int half = c >= 18, cc = c - half * 18, grp = pn * 9 + (cc >> 1), col = (cc & 1) * 8;
    const unsigned short* base = (const unsigned short*)(half ? wu : wg);
    int o = grp * 16;
    if (grp >= 224) { base = bt; o = half * 1024 + (grp - 224) * 16; }
    bsrc[i] = base + (k * LDW + col + o);
  }
#define G1_STAGE_LOADS(buf) do { \
    char* sa_ = smem + (buf) * G1_STAGE; char* sb_ = sa_ + G1_A_BYTES; \
    _Pragma("unroll") for (int i = 0; i < 2; ++i) { GLDS16(xn + aoffs[i], sa_ + (i * 8 + wid) * 1024); aoffs[i] += G1_BK; } \
    _Pragma("unroll") for (int i = 0; i < 2; ++i) { GLDS16(bsrc[i], sb_ + (i * 8 + wid) * 1024); bsrc[i] += G1_BK * LDW; } \
    if (wid < 2) { GLDS16(bsrc[2], sb_ + (16 + wid) * 1024); bsrc[2] += G1_BK * LDW; } \
  } while (0)
  f32x4 acc[2][18];
#pragma unroll
  for (int i = 0; i < 2; ++i)
#pragma unroll
    for (int j = 0; j < 18; ++j) acc[i][j] = (f32x4){0.f, 0.f, 0.f, 0.f};

  const int fr = lane & 15, fq = lane >> 4, tq = (lane & 15) >> 2, tp = lane & 3;
  const int ob_ = fr * 64 + fq * 16, aoff0 = (wid * 2) * 1024 + (ob_ ^ (((ob_ >> 9) & 1) << 5));
  const int bRow = G1_A_BYTES + ((8 * fq + tq) * G1_CH + (tp >> 1)) * 16 + 8 * (tp & 1);
  const int bEven = bRow + 32 * (fq & 1), bOdd = bRow - 32 * (fq & 1);

  const unsigned ldsb = (unsigned)(size_t)(__attribute__((address_space(3))) char*)smem;
  const unsigned aA = ldsb + aoff0, aE = ldsb + bEven, aO = ldsb + bOdd;
#define G1_RDB(dst, buf, jg) do { \
    dst[0] = join_frag(lds_tr64<32 * ((jg) * 6 + 0)>(aE + (buf) * G1_STAGE), lds_tr64<32 * ((jg) * 6 + 0) + 4 * G1_CH * 16>(aE + (buf) * G1_STAGE)); \
    dst[1] = join_frag(lds_tr64<32 * ((jg) * 6 + 1)>(aO + (buf) * G1_STAGE), lds_tr64<32 * ((jg) * 6 + 1) + 4 * G1_CH * 16>(aO + (buf) * G1_STAGE)); \
    dst[2] = join_frag(lds_tr64<32 * ((jg) * 6 + 2)>(aE + (buf) * G1_STAGE), lds_tr64<32 * ((jg) * 6 + 2) + 4 * G1_CH * 16>(aE + (buf) * G1_STAGE)); \
    dst[3] = join_frag(lds_tr64<32 * ((jg) * 6 + 3)>(aO + (buf) * G1_STAGE), lds_tr64<32 * ((jg) * 6 + 3) + 4 * G1_CH * 16>(aO + (buf) * G1_STAGE)); \
    dst[4] = join_frag(lds_tr64<32 * ((jg) * 6 + 4)>(aE + (buf) * G1_STAGE), lds_tr64<32 * ((jg) * 6 + 4) + 4 * G1_CH * 16>(aE + (buf) * G1_STAGE)); \
    dst[5] = join_frag(lds_tr64<32 * ((jg) * 6 + 5)>(aO + (buf) * G1_STAGE), lds_tr64<32 * ((jg) * 6 + 5) + 4 * G1_CH * 16>(aO + (buf) * G1_STAGE)); \
  } while (0)
#define G1_MMA(src, jg) do { \
    _Pragma("unroll") for (int jj = 0; jj < 6; ++jj) \
      _Pragma("unroll") for (int mi = 0; mi < 2; ++mi) \
        acc[mi][(jg) * 6 + jj] = __builtin_amdgcn_mfma_f32_16x16x32_bf16(af[mi], src[jj], acc[mi][(jg) * 6 + jj], 0, 0, 0); \
  } while (0)
#define G1_COMPUTE(buf) do { \
    bf16x8 af[2], b0[6], b1[6]; \
    af[0] = lds_b128<0>(aA + (buf) * G1_STAGE); af[1] = lds_b128<1024>(aA + (buf) * G1_STAGE); \
    G1_RDB(b0, buf, 0); \
    WAIT_LGKM(0); \
    G1_RDB(b1, buf, 1); \
    G1_MMA(b0, 0); \
    WAIT_LGKM(0); \
    G1_RDB(b0, buf, 2); \
    G1_MMA(b1, 1); \
    WAIT_LGKM(0); \
    G1_MMA(b0, 2); \
  } while (0)

  G1_STAGE_LOADS(0); G1_STAGE_LOADS(1); G1_STAGE_LOADS(2);
#define G1_STEP(u) do { if (wid < 2) WAIT_VM(10); else WAIT_VM(8); RAW_BAR(); G1_STAGE_LOADS(((u) + 3) & 3); G1_COMPUTE(u); } while (0)
  for (int kt0 = 0; kt0 < G1_NK - 4; kt0 += 4) {
    G1_STEP(0); G1_STEP(1); G1_STEP(2); G1_STEP(3);
  }
  if (wid < 2) WAIT_VM(10); else WAIT_VM(8);
  RAW_BAR(); G1_STAGE_LOADS(3); G1_COMPUTE(0);
  if (wid < 2) WAIT_VM(10); else WAIT_VM(8);
  RAW_BAR(); G1_COMPUTE(1);
  if (wid < 2) WAIT_VM(5); else WAIT_VM(4);
  RAW_BAR(); G1_COMPUTE(2);
  WAIT_VM(0);
  RAW_BAR(); G1_COMPUTE(3);
  int tz = threadIdx.x; asm volatile("" : "+v"(tz));
  const int lane2 = tz & 63, fr2 = tz & 15, fq2 = (tz >> 4) & 3;
  unsigned short* ah = (unsigned short*)(ws + OFF_AH);
  float2* bu = (float2*)(ws + OFF_BU);
  const float2* coef = (const float2*)(ws + OFF_COEF);
  const float2* lamb = (const float2*)(ws + OFF_LAM);
  float2* Eo = (float2*)(ws + OFF_E);
  const int nf = min(9, max(0, 224 - pn * 9));
  WAIT_VM(0); RAW_BAR();
  constexpr int EP_LD = 304;
  char* wl = smem + wid * (32 * EP_LD);
#pragma unroll
  for (int j = 0; j < 9; ++j) {
    if (j < nf) {
#pragma unroll
      for (int mi = 0; mi < 2; ++mi)
#pragma unroll
        for (int r = 0; r < 4; ++r) {
          float g = rbf(acc[mi][j][r]), u = rbf(acc[mi][9 + j][r]);
          float a = rbf(gelu_tanh_f(g)) * u;
          *(unsigned short*)(wl + (mi * 16 + fq2 * 4 + r) * EP_LD + (j * 16 + fr2) * 2) = f2bf(a);
        }
    } else {
      const int p = (pn * 9 + j - 224) * 16 + fr2;
      const float2 cf = coef[p];
      const float2 lam = lamb[p];
      float er[2], ei[2];
#pragma unroll
      for (int mi = 0; mi < 2; ++mi) {
        er[mi] = 0.f; ei[mi] = 0.f;
#pragma unroll
        for (int r = 0; r < 4; ++r) {
          const int row = brow + wid * 32 + mi * 16 + fq2 * 4 + r;
          float ur = acc[mi][j][r], ui = acc[mi][9 + j][r];
          const float br = cf.x * ur - cf.y * ui, bi = cf.x * ui + cf.y * ur;
          bu[(size_t)row * PP + p] = make_float2(br, bi);
          const float nr = lam.x * er[mi] - lam.y * ei[mi] + br, ni = lam.x * ei[mi] + lam.y * er[mi] + bi;
          er[mi] = nr; ei[mi] = ni;
        }
      }
      const float l2r = lam.x * lam.x - lam.y * lam.y, l2i = 2.f * lam.x * lam.y;
      const float l4r = l2r * l2r - l2i * l2i, l4i = 2.f * l2r * l2i;
      const float l8r = l4r * l4r - l4i * l4i, l8i = 2.f * l4r * l4i;
      const float l16r = l8r * l8r - l8i * l8i, l16i = 2.f * l8r * l8i;
      float tr = l16r * er[0] - l16i * ei[0] + er[1], ti = l16r * ei[0] + l16i * er[0] + ei[1];
      float mr = (fq2 & 1) ? 1.f : l4r, mi_ = (fq2 & 1) ? 0.f : l4i;
      if (!(fq2 & 2)) { const float a = mr * l8r - mi_ * l8i, b = mr * l8i + mi_ * l8r; mr = a; mi_ = b; }
      float sr = mr * tr - mi_ * ti, si = mr * ti + mi_ * tr;
      sr += __shfl_xor(sr, 16); si += __shfl_xor(si, 16);
      sr += __shfl_xor(sr, 32); si += __shfl_xor(si, 32);
      if (fq2 == 0) Eo[(size_t)((brow >> 5) + wid) * PP + p] = make_float2(sr, si);
    }
  }
  asm volatile("s_waitcnt lgkmcnt(0)" ::: "memory");
  {
    const int nch = nf * 2;
    unsigned short* dst0 = ah + (size_t)(brow + wid * 32) * KAH + pn * 144;
#pragma unroll
    for (int i = 0; i < 9; ++i) {
      const int q = i * 64 + lane2, row = q / 18, ch = q % 18;
      if (ch < nch) {
        bf16x8 v = *(const bf16x8*)(wl + row * EP_LD + ch * 16);
        *(bf16x8*)(dst0 + (size_t)row * KAH + ch * 8) = v;
      }
    }
  }
}

__global__ __launch_bounds__(256) void scanA_kernel(char* __restrict__ ws) {
  const int p = (blockIdx.x & 3) * 256 + threadIdx.x, c = blockIdx.x >> 2;
  const float2 lam = ((const float2*)(ws + OFF_LAM))[p];
  const float2* bu = (const float2*)(ws + OFF_BU) + (size_t)c * SCAN_T * PP + p;
  float er = 0.f, ei = 0.f;
#pragma unroll 8
  for (int s = 0; s < SCAN_T; ++s) {
    float2 b = bu[(size_t)s * PP];
    float nr = lam.x * er - lam.y * ei + b.x, ni = lam.x * ei + lam.y * er + b.y;
    er = nr; ei = ni;
  }
  ((float2*)(ws + OFF_E))[(size_t)c * PP + p] = make_float2(er, ei);
}

__global__ __launch_bounds__(256) void scanC_kernel(char* __restrict__ ws) {
  const int p = (blockIdx.x & 3) * 256 + threadIdx.x, c = blockIdx.x >> 2;
  const float2* bu = (const float2*)(ws + OFF_BU) + (size_t)c * SCAN_T * PP + p;
  float2 b[SCAN_T];
#pragma unroll
  for (int s = 0; s < SCAN_T; ++s) b[s] = bu[(size_t)s * PP];
  const float2 lam = ((const float2*)(ws + OFF_LAM))[p];
  const float2* E = (const float2*)(ws + OFF_E) + p;
  float lr = lam.x, li = lam.y;
#pragma unroll
  for (int i = 0; i < 5; ++i) { float a = lr * lr - li * li, bb = 2.f * lr * li; lr = a; li = bb; }
  float hr = 0.f, hi = 0.f;
  {
    float2 e[NCHUNK - 1];
#pragma unroll
    for (int u = 0; u < NCHUNK - 1; ++u) e[u] = (u < c) ? E[(size_t)u * PP] : make_float2(0.f, 0.f);
#pragma unroll
    for (int u = 0; u < NCHUNK - 1; ++u) if (u < c) { float nr = lr * hr - li * hi + e[u].x, ni = lr * hi + li * hr + e[u].y; hr = nr; hi = ni; }
  }
  unsigned short* ah = (unsigned short*)(ws + OFF_AH) + (size_t)c * SCAN_T * KAH + FF + p;
#pragma unroll
  for (int s = 0; s < SCAN_T; ++s) {
    float nr = lam.x * hr - lam.y * hi + b[s].x, ni = lam.x * hi + lam.y * hr + b[s].y;
    hr = nr; hi = ni;
    ah[(size_t)s * KAH] = f2bf(hr);
    ah[(size_t)s * KAH + PP] = f2bf(hi);
  }
}

constexpr int G2_BM = 256, G2_BN = 128, G2_CH = G2_BN / 8;
constexpr int G2_A_BYTES = G2_BM * 64 * 2, G2_B_BYTES = 64 * G2_BN * 2, G2_STAGE = G2_A_BYTES + G2_B_BYTES;
constexpr int G2_NST = 3, G2_STEPS = 22;
__device__ __forceinline__ int g2_swz(int k) { return ((k & 3) | (((k >> 3) & 1) << 2)) << 1; }

__global__ __launch_bounds__(512, 2) void gemm2_kernel(
    const bf16* __restrict__ x, const bf16* __restrict__ wd, const bf16* __restrict__ dskip,
    char* __restrict__ ws, float* __restrict__ out) {
  extern __shared__ __attribute__((aligned(16))) char smem[];
  const int tid = threadIdx.x, lane = tid & 63, wid = __builtin_amdgcn_readfirstlane(tid >> 6);
  const int bid = blockIdx.x;
  const int xcd = bid & 7, idx = bid >> 3;
  const int split = idx & 3, pn = idx >> 2, pm = xcd;
  const int tile = pm * 8 + pn;
  const int brow = pm * G2_BM, bcol = pn * G2_BN;
  const int kt0 = split * G2_STEPS;
  const int n_d = split < 2 ? G2_STEPS : (split == 2 ? 12 : 0);
  const unsigned short* ah = (const unsigned short*)(ws + OFF_AH);
  const unsigned short* ct = (const unsigned short*)(ws + OFF_CT);
  const unsigned short* asrc[4];
#pragma unroll
  for (int i = 0; i < 4; ++i) {
    int R = (i * 8 + wid) * 8 + (lane >> 3), C = ((lane & 7) ^ (R & 7)) * 8;
    asrc[i] = ah + (size_t)(brow + R) * KAH + kt0 * 64 + C;
  }
  int boffs[2];
#pragma unroll
  for (int i = 0; i < 2; ++i) {
    int Lc = (i * 8 + wid) * 64 + lane, k = Lc / G2_CH, cp = Lc % G2_CH, c = cp ^ g2_swz(k);
    boffs[i] = k * DM + bcol + c * 8;
  }
#define G2_STAGE_LOADS(buf, kt) do { \
    char* sa_ = smem + (buf) * G2_STAGE; char* sb_ = sa_ + G2_A_BYTES; \
    _Pragma("unroll") for (int i = 0; i < 4; ++i) { GLDS16(asrc[i], sa_ + (i * 8 + wid) * 1024); asrc[i] += 64; } \
    const unsigned short* bb_ = (kt) < 56 ? (const unsigned short*)wd + (size_t)(kt) * 64 * DM : ct + (size_t)((kt) - 56) * 64 * DM; \
    _Pragma("unroll") for (int i = 0; i < 2; ++i) GLDS16(bb_ + boffs[i], sb_ + (i * 8 + wid) * 1024); \
  } while (0)
  const int wr = wid >> 1, wc = wid & 1;
  const int fr = lane & 15, fq = lane >> 4, tq = (lane & 15) >> 2, tp = lane & 3;
  f32x4 accd[4][4], accy[4][4];
#pragma unroll
  for (int i = 0; i < 4; ++i)
#pragma unroll
    for (int j = 0; j < 4; ++j) { accd[i][j] = (f32x4){0.f, 0.f, 0.f, 0.f}; accy[i][j] = (f32x4){0.f, 0.f, 0.f, 0.f}; }

  const unsigned ldsb = (unsigned)(size_t)(__attribute__((address_space(3))) char*)smem;
  const unsigned aA0 = ldsb + (wr * 64 + fr) * 128 + ((fq ^ (fr & 7)) * 16);
  const unsigned aA1 = ldsb + (wr * 64 + fr) * 128 + (((4 + fq) ^ (fr & 7)) * 16);
  unsigned aB[4][2];
#pragma unroll
  for (int nj = 0; nj < 4; ++nj)
#pragma unroll
    for (int t = 0; t < 2; ++t) {
      const int k0 = 8 * fq + tq + 4 * t, j = wc * 4 + nj;
      aB[nj][t] = ldsb + G2_A_BYTES + (k0 * G2_CH + ((2 * j + (tp >> 1)) ^ g2_swz(k0))) * 16 + 8 * (tp & 1);
    }
#define G2_RD(af, bfm, bo, kk) do { \
    _Pragma("unroll") for (int nj = 0; nj < 4; ++nj) \
      bfm[nj] = join_frag(lds_tr64<(kk) * 32 * G2_CH * 16>(aB[nj][0] + (bo)), lds_tr64<(kk) * 32 * G2_CH * 16>(aB[nj][1] + (bo))); \
    af[0] = lds_b128<0>(((kk) ? aA1 : aA0) + (bo)); af[1] = lds_b128<2048>(((kk) ? aA1 : aA0) + (bo)); \
    af[2] = lds_b128<4096>(((kk) ? aA1 : aA0) + (bo)); af[3] = lds_b128<6144>(((kk) ? aA1 : aA0) + (bo)); \
  } while (0)
#define G2_MMA(acc, af, bfm) do { \
    _Pragma("unroll") for (int mi = 0; mi < 4; ++mi) \
      _Pragma("unroll") for (int nj = 0; nj < 4; ++nj) acc[mi][nj] = __builtin_amdgcn_mfma_f32_16x16x32_bf16(af[mi], bfm[nj], acc[mi][nj], 0, 0, 0); \
  } while (0)
#define G2_COMPUTE(bo, acc) do { \
    bf16x8 a0[4], a1[4], f0[4], f1[4]; \
    G2_RD(a0, f0, bo, 0); \
    WAIT_LGKM(0); \
    G2_RD(a1, f1, bo, 1); \
    G2_MMA(acc, a0, f0); \
    WAIT_LGKM(0); \
    G2_MMA(acc, a1, f1); \
  } while (0)
#define G2_ITER(acc) do { \
    if (it + 1 < G2_STEPS) WAIT_VM(6); else WAIT_VM(0); \
    RAW_BAR(); \
    if (it + 2 < G2_STEPS) { int nb = cb + 2; if (nb >= 3) nb -= 3; G2_STAGE_LOADS(nb, kt0 + it + 2); } \
    const unsigned bo = cb * G2_STAGE; \
    G2_COMPUTE(bo, acc); \
    cb = cb + 1; if (cb == 3) cb = 0; \
  } while (0)

  G2_STAGE_LOADS(0, kt0); G2_STAGE_LOADS(1, kt0 + 1);
  int it = 0, cb = 0;
  for (; it < n_d; ++it) G2_ITER(accd);
  for (; it < G2_STEPS; ++it) G2_ITER(accy);

  f32x4* part = (f32x4*)(ws + OFF_PART) + (size_t)tile * 5 * 8192;
  const int pbase = wid * 1024 + lane;
  if (split < 3) {
    f32x4* dst = part + (size_t)split * 8192 + pbase;
#pragma unroll
    for (int mi = 0; mi < 4; ++mi)
#pragma unroll
      for (int nj = 0; nj < 4; ++nj) dst[(mi * 4 + nj) * 64] = accd[mi][nj];
  }
  if (split >= 2) {
    f32x4* dst = part + (size_t)(split + 1) * 8192 + pbase;
#pragma unroll
    for (int mi = 0; mi < 4; ++mi)
#pragma unroll
      for (int nj = 0; nj < 4; ++nj) dst[(mi * 4 + nj) * 64] = accy[mi][nj];
  }
  asm volatile("s_waitcnt vmcnt(0)" ::: "memory");
  __syncthreads();
  int* flag = (int*)smem;
  if (tid == 0) {
    __builtin_amdgcn_fence(__ATOMIC_RELEASE, "agent");
    asm volatile("s_waitcnt vmcnt(0)" ::: "memory");
    const unsigned old = __hip_atomic_fetch_add((unsigned*)(ws + OFF_CNT) + tile, 1u, __ATOMIC_RELAXED, __HIP_MEMORY_SCOPE_AGENT);
    if (old == 3u) {
      __builtin_amdgcn_fence(__ATOMIC_ACQUIRE, "agent");
      asm volatile("s_waitcnt vmcnt(0)" ::: "memory");
    }
    *flag = (int)old;
  }
  __syncthreads();
  const int arrived = *flag;
  __syncthreads();
  if (arrived != 3) return;
#pragma unroll
  for (int sl = 0; sl < 5; ++sl) {
    const bool own = (sl == split) || (sl == split + 1 && split >= 2);
    const bool ownd = (sl < 3) && (sl == split);
    const bool owny = (sl >= 3) && (sl == split + 1);
    (void)own;
    if (ownd || owny) continue;
    const f32x4* src = part + (size_t)sl * 8192 + pbase;
    if (sl < 3) {
#pragma unroll
      for (int mi = 0; mi < 4; ++mi)
#pragma unroll
        for (int nj = 0; nj < 4; ++nj) accd[mi][nj] += src[(mi * 4 + nj) * 64];
    } else {
#pragma unroll
      for (int mi = 0; mi < 4; ++mi)
#pragma unroll
        for (int nj = 0; nj < 4; ++nj) accy[mi][nj] += src[(mi * 4 + nj) * 64];
    }
  }
  const unsigned short* xs = (const unsigned short*)x;
  const unsigned short* xn = (const unsigned short*)(ws + OFF_XN);
  constexpr int EP_LD = 32 * 8 + 16;
  char* wl = smem + 16 + wid * (64 * EP_LD);
#pragma unroll
  for (int h = 0; h < 2; ++h) {
#pragma unroll
    for (int n2 = 0; n2 < 2; ++n2)
#pragma unroll
      for (int mi = 0; mi < 4; ++mi)
#pragma unroll
        for (int r = 0; r < 4; ++r) {
          const float dn = rbf(accd[mi][h * 2 + n2][r]);
          const float g = rbf(dn * __builtin_amdgcn_rcpf(1.0f + __expf(-dn)));
          *(float2*)(wl + (mi * 16 + fq * 4 + r) * EP_LD + (n2 * 16 + fr) * 8) = make_float2(2.0f * accy[mi][h * 2 + n2][r], g);
        }
    asm volatile("s_waitcnt lgkmcnt(0)" ::: "memory");
    const int c4 = (lane & 7) * 4, rsub = lane >> 3;
    const int col = bcol + wc * 64 + h * 32 + c4;
    const bf16x4 dk4 = *(const bf16x4*)((const unsigned short*)dskip + col);
#pragma unroll
    for (int i = 0; i < 8; ++i) {
      const int rl = i * 8 + rsub, row = brow + wr * 64 + rl;
      const size_t o = (size_t)row * DM + col;
      const bf16x4 x4 = *(const bf16x4*)(xs + o), n4 = *(const bf16x4*)(xn + (size_t)row * LDXN + col);
      const f32x4 p0 = *(const f32x4*)(wl + rl * EP_LD + c4 * 8), p1 = *(const f32x4*)(wl + rl * EP_LD + c4 * 8 + 16);
      f32x4 res;
      res[0] = bf2f((unsigned short)x4[0]) + (p0[0] + rbf(bf2f((unsigned short)n4[0]) * bf2f((unsigned short)dk4[0]))) * p0[1];
      res[1] = bf2f((unsigned short)x4[1]) + (p0[2] + rbf(bf2f((unsigned short)n4[1]) * bf2f((unsigned short)dk4[1]))) * p0[3];
      res[2] = bf2f((unsigned short)x4[2]) + (p1[0] + rbf(bf2f((unsigned short)n4[2]) * bf2f((unsigned short)dk4[2]))) * p1[1];
      res[3] = bf2f((unsigned short)x4[3]) + (p1[2] + rbf(bf2f((unsigned short)n4[3]) * bf2f((unsigned short)dk4[3]))) * p1[3];
      *(f32x4*)(out + o) = res;
    }
    asm volatile("s_waitcnt lgkmcnt(0)" ::: "memory");
  }
}

extern "C" void kernel_launch(void* const* d_in, const int* in_sizes, int n_in,
                              void* d_out, int out_size, void* d_ws, size_t ws_size,
                              hipStream_t stream) {
  const bf16* x = (const bf16*)d_in[0];
  const bf16* lam_re = (const bf16*)d_in[1];
  const bf16* lam_im = (const bf16*)d_in[2];
  const bf16* b_re = (const bf16*)d_in[3];
  const bf16* b_im = (const bf16*)d_in[4];
  const bf16* c_re = (const bf16*)d_in[5];
  const bf16* c_im = (const bf16*)d_in[6];
  const bf16* log_dt = (const bf16*)d_in[7];
  const bf16* dskip = (const bf16*)d_in[8];
  const bf16* wg = (const bf16*)d_in[10];
  const bf16* wu = (const bf16*)d_in[11];
  const bf16* wd = (const bf16*)d_in[12];
  char* ws = (char*)d_ws;
  float* out = (float*)d_out;

  hipFuncSetAttribute((const void*)gemm1_kernel, hipFuncAttributeMaxDynamicSharedMemorySize, G1_NST * G1_STAGE);
  hipFuncSetAttribute((const void*)gemm2_kernel, hipFuncAttributeMaxDynamicSharedMemorySize, G2_NST * G2_STAGE);

  prep_kernel<<<PREP_BLOCKS, 256, 0, stream>>>(x, lam_re, lam_im, b_re, b_im, c_re, c_im, log_dt, ws);
  gemm1_kernel<<<256, 512, G1_NST * G1_STAGE, stream>>>(wg, wu, ws);
  scanC_kernel<<<4 * NCHUNK, 256, 0, stream>>>(ws);
  gemm2_kernel<<<256, 512, G2_NST * G2_STAGE, stream>>>(x, wd, dskip, ws, out);
}
```

```cpp
#include <hip/hip_runtime.h>
#include <hip/hip_bf16.h>
#include <stdint.h>

typedef __hip_bfloat16 bf16;
typedef __attribute__((ext_vector_type(8))) short bf16x8;
typedef __attribute__((ext_vector_type(4))) short bf16x4;
typedef __attribute__((ext_vector_type(4))) float f32x4;
typedef __attribute__((__vector_size__(4 * sizeof(short)))) short v4s;

constexpr int L_SEQ = 2048, DM = 1024, FF = 3584, PP = 1024;
constexpr int LDW = 3584;
constexpr int LDXN = DM + 64;
constexpr int KAH = FF + 2 * PP + 64;
constexpr int SCAN_T = 32, NCHUNK = L_SEQ / SCAN_T;

constexpr size_t OFF_XN   = 0;
constexpr size_t OFF_BT   = OFF_XN + (size_t)L_SEQ * LDXN * 2;
constexpr size_t OFF_CT   = OFF_BT + (size_t)DM * LDW * 2;
constexpr size_t OFF_AH   = OFF_CT + (size_t)2 * PP * DM * 2;
constexpr size_t OFF_BU   = OFF_AH + (size_t)L_SEQ * KAH * 2;
constexpr size_t OFF_E    = OFF_BU + (size_t)L_SEQ * PP * 8;
constexpr size_t OFF_LAM  = OFF_E + (size_t)NCHUNK * PP * 8;
constexpr size_t OFF_COEF = OFF_LAM + (size_t)PP * 8;

__device__ __forceinline__ float bf2f(unsigned short u) { return __uint_as_float(((unsigned)u) << 16); }
__device__ __forceinline__ unsigned short f2bf(float f) {
  __bf16 b = (__bf16)f;
  return __builtin_bit_cast(unsigned short, b);
}
__device__ __forceinline__ float rbf(float f) { return bf2f(f2bf(f)); }

constexpr int PREP_LN_BLOCKS = L_SEQ / 4;
constexpr int PREP_TR_BLOCKS = 4 * (1024 / 64) * (1024 / 64);
constexpr int PREP_BLOCKS = PREP_LN_BLOCKS + PREP_TR_BLOCKS + 4;

__global__ __launch_bounds__(256) void prep_kernel(
    const bf16* __restrict__ x, const bf16* __restrict__ lam_re, const bf16* __restrict__ lam_im,
    const bf16* __restrict__ b_re, const bf16* __restrict__ b_im,
    const bf16* __restrict__ c_re, const bf16* __restrict__ c_im,
    const bf16* __restrict__ log_dt, char* __restrict__ ws) {
  __shared__ unsigned short tile[64][66];
  const int bid = blockIdx.x, tid = threadIdx.x;
  if (bid < PREP_LN_BLOCKS) {
    const int wave = tid >> 6, lane = tid & 63;
    const int row = bid * 4 + wave;
    const unsigned short* xr = (const unsigned short*)x + (size_t)row * DM;
    float v[16];
    bf16x8 a = *(const bf16x8*)(xr + lane * 8);
    bf16x8 b = *(const bf16x8*)(xr + 512 + lane * 8);
    float sum = 0.f;
#pragma unroll
    for (int j = 0; j < 8; ++j) { v[j] = bf2f((unsigned short)a[j]); v[8 + j] = bf2f((unsigned short)b[j]); }
#pragma unroll
    for (int j = 0; j < 16; ++j) sum += v[j];
#pragma unroll
    for (int o = 32; o > 0; o >>= 1) sum += __shfl_xor(sum, o);
    const float mean = sum * (1.0f / DM);
    float sq = 0.f;
#pragma unroll
    for (int j = 0; j < 16; ++j) { float d = v[j] - mean; sq += d * d; }
#pragma unroll
    for (int o = 32; o > 0; o >>= 1) sq += __shfl_xor(sq, o);
    const float var_b = rbf(sq * (1.0f / DM));
    const float mean_b = rbf(mean);
    const float den = rbf(sqrtf(rbf(var_b + 1e-5f)));
    bf16x8 oa, ob;
#pragma unroll
    for (int j = 0; j < 8; ++j) {
      oa[j] = (short)f2bf(rbf(v[j] - mean_b) / den);
      ob[j] = (short)f2bf(rbf(v[8 + j] - mean_b) / den);
    }
    unsigned short* xo = (unsigned short*)(ws + OFF_XN) + (size_t)row * LDXN;
    *(bf16x8*)(xo + lane * 8) = oa;
    *(bf16x8*)(xo + 512 + lane * 8) = ob;
  } else if (bid < PREP_LN_BLOCKS + PREP_TR_BLOCKS) {
    const int t = bid - PREP_LN_BLOCKS;
    const int mat = t >> 8, ti = (t >> 4) & 15, tj = t & 15;
    const unsigned short* src = (const unsigned short*)(mat == 0 ? b_re : mat == 1 ? b_im : mat == 2 ? c_re : c_im);
    const int tc = tid & 7, tr = tid >> 3;
#pragma unroll
    for (int i = 0; i < 2; ++i) {
      const int r = tr + 32 * i;
      bf16x8 v = *(const bf16x8*)(src + (size_t)(ti * 64 + r) * 1024 + tj * 64 + tc * 8);
#pragma unroll
      for (int e = 0; e < 8; ++e) tile[r][tc * 8 + e] = (unsigned short)v[e];
    }
    __syncthreads();
    unsigned short* dst; size_t ld; unsigned short neg = 0;
    if (mat < 2) { dst = (unsigned short*)(ws + OFF_BT) + mat * 1024; ld = LDW; }
    else { dst = (unsigned short*)(ws + OFF_CT) + (size_t)(mat - 2) * 1024 * DM; ld = DM; neg = (mat == 3) ? 0x8000 : 0; }
#pragma unroll
    for (int i = 0; i < 2; ++i) {
      const int c = tr + 32 * i;
      bf16x8 v;
#pragma unroll
      for (int e = 0; e < 8; ++e) v[e] = (short)(tile[tc * 8 + e][c] ^ neg);
      *(bf16x8*)(dst + (size_t)(tj * 64 + c) * ld + ti * 64 + tc * 8) = v;
    }
  } else {
    const int p = (bid - PREP_LN_BLOCKS - PREP_TR_BLOCKS) * 256 + tid;
    const float lr = bf2f(((const unsigned short*)lam_re)[p]);
    const float li = bf2f(((const unsigned short*)lam_im)[p]);
    const float dt = rbf(expf(bf2f(((const unsigned short*)log_dt)[p])));
    const float ar = dt * lr, ai = dt * li;
    const float e = expf(ar);
    float sn, cs; sincosf(ai, &sn, &cs);
    const float br_ = e * cs, bi_ = e * sn;
    const float nr = br_ - 1.0f, ni = bi_;
    const float den = lr * lr + li * li;
    const float cr = (nr * lr + ni * li) / den, ci = (ni * lr - nr * li) / den;
    ((float2*)(ws + OFF_LAM))[p] = make_float2(br_, bi_);
    ((float2*)(ws + OFF_COEF))[p] = make_float2(cr, ci);
  }
}

__device__ __forceinline__ int a_lds_byte(int r, int c) {
  int st = (r >> 4) * 2 + (c >> 5), rr = r & 15, cc = c & 31, ob = rr * 64 + cc * 2;
  return st * 1024 + (ob ^ (((ob >> 9) & 1) << 5));
}
__device__ __forceinline__ void a_stage_rc(int b, int& R, int& C) {
  int st = b >> 10, sb = b & 1023, swz = sb ^ (((sb >> 9) & 1) << 5);
  R = (st >> 1) * 16 + (swz >> 6); C = (st & 1) * 32 + ((swz & 63) >> 1);
}
__device__ __forceinline__ bf16x8 tr_frag(const char* lds_lo, const char* lds_hi) {
  v4s lo = __builtin_amdgcn_ds_read_tr16_b64_v4i16((__attribute__((address_space(3))) v4s*)(lds_lo));
  v4s hi = __builtin_amdgcn_ds_read_tr16_b64_v4i16((__attribute__((address_space(3))) v4s*)(lds_hi));
  bf16x8 r;
  r[0] = lo[0]; r[1] = lo[1]; r[2] = lo[2]; r[3] = lo[3];
  r[4] = hi[0]; r[5] = hi[1]; r[6] = hi[2]; r[7] = hi[3];
  return r;
}
typedef __attribute__((ext_vector_type(2))) unsigned u32x2;
typedef __attribute__((ext_vector_type(4))) unsigned u32x4;
template <int OFF> __device__ __forceinline__ u32x2 lds_tr64(unsigned addr) {
  u32x2 r; asm volatile("ds_read_b64_tr_b16 %0, %1 offset:%2" : "=v"(r) : "v"(addr), "i"(OFF) : "memory"); return r;
}
template <int OFF> __device__ __forceinline__ bf16x8 lds_b128(unsigned addr) {
  u32x4 r; asm volatile("ds_read_b128 %0, %1 offset:%2" : "=v"(r) : "v"(addr), "i"(OFF) : "memory"); return __builtin_bit_cast(bf16x8, r);
}
__device__ __forceinline__ bf16x8 join_frag(u32x2 lo, u32x2 hi) {
  u32x4 r = __builtin_shufflevector(lo, hi, 0, 1, 2, 3); return __builtin_bit_cast(bf16x8, r);
}
#define WAIT_LGKM(n) do { asm volatile("s_waitcnt lgkmcnt(" #n ")" ::: "memory"); __builtin_amdgcn_sched_barrier(0); } while (0)
#define GLDS16(gptr, ldsptr) __builtin_amdgcn_global_load_lds((const void __attribute__((address_space(1)))*)(gptr), (void __attribute__((address_space(3)))*)(ldsptr), 16, 0, 0)

__device__ __forceinline__ float gelu_tanh_f(float x) {
  const float k0 = 0.7978845608028654f, k1 = 0.044715f;
  float u = k0 * (x + k1 * x * x * x);
  return x * __builtin_amdgcn_rcpf(1.0f + __expf(-2.0f * u));
}

constexpr int G1_BM = 256, G1_BN = 288, G1_CH = G1_BN / 8;
constexpr int G1_BK = 32, G1_NST = 4, G1_NK = 1024 / G1_BK;
constexpr int G1_A_BYTES = G1_BM * G1_BK * 2, G1_B_BYTES = G1_BK * G1_BN * 2, G1_STAGE = G1_A_BYTES + G1_B_BYTES;
__device__ __forceinline__ int g1_swz(int k) { return ((k >> 3) & 1) << 1; }
#define WAIT_VM(n) asm volatile("s_waitcnt vmcnt(" #n ") lgkmcnt(0)" ::: "memory")
#define RAW_BAR() __builtin_amdgcn_s_barrier()

__global__ __launch_bounds__(512, 2) void gemm1_kernel(
    const bf16* __restrict__ wg, const bf16* __restrict__ wu, char* __restrict__ ws) {
  extern __shared__ __attribute__((aligned(16))) char smem[];
  const int tid = threadIdx.x, lane = tid & 63, wid = __builtin_amdgcn_readfirstlane(tid >> 6);
  const int bid = blockIdx.x;
  const int xcd = bid & 7, idx = bid >> 3;
  const int pn = xcd * 4 + (idx & 3), pm = idx >> 2;
  const int brow = pm * G1_BM;
  const int wr = wid & 3, wc = wid >> 2;
  const unsigned short* xn = (const unsigned short*)(ws + OFF_XN);
  const unsigned short* bt = (const unsigned short*)(ws + OFF_BT);

  unsigned aoffs[2];
#pragma unroll
  for (int i = 0; i < 2; ++i) {
    int sb = lane * 16, swz = sb ^ (((sb >> 9) & 1) << 5);
    int R = (i * 8 + wid) * 16 + (swz >> 6), C = (swz & 63) >> 1;
    aoffs[i] = (unsigned)((brow + R) * LDXN + C);
  }
  const unsigned short* bsrc[3];
#pragma unroll
  for (int i = 0; i < 3; ++i) {
    int inst = i * 8 + wid; if (inst > 17) inst = 17;
    int Lc = inst * 64 + lane, k = Lc / G1_CH, cp = Lc % G1_CH, c = cp ^ g1_swz(k);
    int half = c & 1, P = pn * 144 + (c >> 1) * 8;
    const unsigned short* base = (const unsigned short*)(half ? wu : wg);
    int o = P;
    if (P >= FF) { base = bt; o = half * 1024 + (P - FF); }
    bsrc[i] = base + (k * LDW + o);
  }
#define G1_STAGE_LOADS(buf) do { \
    char* sa_ = smem + (buf) * G1_STAGE; char* sb_ = sa_ + G1_A_BYTES; \
    _Pragma("unroll") for (int i = 0; i < 2; ++i) { GLDS16(xn + aoffs[i], sa_ + (i * 8 + wid) * 1024); aoffs[i] += G1_BK; } \
    _Pragma("unroll") for (int i = 0; i < 2; ++i) { GLDS16(bsrc[i], sb_ + (i * 8 + wid) * 1024); bsrc[i] += G1_BK * LDW; } \
    if (wid < 2) { GLDS16(bsrc[2], sb_ + (16 + wid) * 1024); bsrc[2] += G1_BK * LDW; } \
  } while (0)
  f32x4 acc[4][9];
#pragma unroll
  for (int i = 0; i < 4; ++i)
#pragma unroll
    for (int j = 0; j < 9; ++j) acc[i][j] = (f32x4){0.f, 0.f, 0.f, 0.f};

  const int fr = lane & 15, fq = lane >> 4, tq = (lane & 15) >> 2, tp = lane & 3;
  const int ob_ = fr * 64 + fq * 16, aoff0 = (wr * 4) * 1024 + (ob_ ^ (((ob_ >> 9) & 1) << 5));
  const int bRow = G1_A_BYTES + ((8 * fq + tq) * G1_CH + (tp >> 1)) * 16 + 8 * (tp & 1) + 32 * 9 * wc;
  const int sgn = 32 * (fq & 1);
  const unsigned ldsb = (unsigned)(size_t)(__attribute__((address_space(3))) char*)smem;
  const unsigned aA = ldsb + aoff0;
  const unsigned aE = ldsb + bRow + (wc ? -sgn : sgn);
  const unsigned aO = ldsb + bRow + (wc ? sgn : -sgn);
#define G1_RDB1(jj, ad) join_frag(lds_tr64<32 * (jj)>((ad) + bo_), lds_tr64<32 * (jj) + 4 * G1_CH * 16>((ad) + bo_))
#define G1_READ(buf) do { \
    const unsigned bo_ = (buf) * G1_STAGE; \
    bfr[0] = G1_RDB1(0, aE); bfr[1] = G1_RDB1(1, aO); bfr[2] = G1_RDB1(2, aE); \
    af[0] = lds_b128<0>(aA + bo_); af[1] = lds_b128<1024>(aA + bo_); af[2] = lds_b128<2048>(aA + bo_); af[3] = lds_b128<3072>(aA + bo_); \
    bfr[3] = G1_RDB1(3, aO); bfr[4] = G1_RDB1(4, aE); bfr[5] = G1_RDB1(5, aO); \
    bfr[6] = G1_RDB1(6, aE); bfr[7] = G1_RDB1(7, aO); bfr[8] = G1_RDB1(8, aE); \
  } while (0)
#define G1_MMA() do { \
    __builtin_amdgcn_s_setprio(1); \
    _Pragma("unroll") for (int jj = 0; jj < 9; ++jj) \
      _Pragma("unroll") for (int mi = 0; mi < 4; ++mi) \
        acc[mi][jj] = __builtin_amdgcn_mfma_f32_16x16x32_bf16(af[mi], bfr[jj], acc[mi][jj], 0, 0, 0); \
    __builtin_amdgcn_s_setprio(0); \
  } while (0)
#define G1_WAITN(n2, n1) do { if (wid < 2) WAIT_VM(n2); else WAIT_VM(n1); __builtin_amdgcn_sched_barrier(0); } while (0)
#define G1_STEP(u, LOADS, w2, w1) do { \
    G1_READ(u); \
    LOADS; \
    G1_WAITN(w2, w1); RAW_BAR(); \
    G1_MMA(); \
    G1_WAITN(w2, w1); RAW_BAR(); \
  } while (0)

  bf16x8 af[4], bfr[9];
  G1_STAGE_LOADS(0); G1_STAGE_LOADS(1); G1_STAGE_LOADS(2);
  G1_WAITN(10, 8); RAW_BAR();
  if (wc == 1) RAW_BAR();
  for (int kt0 = 0; kt0 < G1_NK - 4; kt0 += 4) {
    G1_STEP(0, G1_STAGE_LOADS(3), 10, 8);
    G1_STEP(1, G1_STAGE_LOADS(0), 10, 8);
    G1_STEP(2, G1_STAGE_LOADS(1), 10, 8);
    G1_STEP(3, G1_STAGE_LOADS(2), 10, 8);
  }
  G1_STEP(0, G1_STAGE_LOADS(3), 10, 8);
  G1_STEP(1, , 5, 4);
  G1_STEP(2, , 0, 0);
  G1_STEP(3, , 0, 0);
  if (wc == 0) RAW_BAR();

  int tz = threadIdx.x; asm volatile("" : "+v"(tz));
  const int lane2 = tz & 63, fr2 = tz & 15, fq2 = (tz >> 4) & 3;
  const bool hi = fr2 >= 8;
  const int f8 = fr2 & 7, rb = fq2 * 4 + (hi ? 2 : 0);
  unsigned short* ah = (unsigned short*)(ws + OFF_AH);
  float2* bu = (float2*)(ws + OFF_BU);
  const float2* coef = (const float2*)(ws + OFF_COEF);
  const float2* lamb = (const float2*)(ws + OFF_LAM);
  float2* Eo = (float2*)(ws + OFF_E);
  const int P0 = pn * 144 + wc * 72;
  const int nf = min(9, max(0, (FF - P0) >> 3));
  RAW_BAR();
  constexpr int EP_LD = 160;
  char* wl = smem + wid * (64 * EP_LD);
#pragma unroll
  for (int jj = 0; jj < 9; ++jj) {
    if (jj < nf) {
#pragma unroll
      for (int mi = 0; mi < 4; ++mi) {
        const f32x4 v = acc[mi][jj];
        const float r0 = __shfl_xor(hi ? v[0] : v[2], 8), r1 = __shfl_xor(hi ? v[1] : v[3], 8);
        const float g0 = rbf(hi ? r0 : v[0]), g1 = rbf(hi ? r1 : v[1]);
        const float u0 = rbf(hi ? v[2] : r0), u1 = rbf(hi ? v[3] : r1);
        char* d = wl + (mi * 16 + rb) * EP_LD + (jj * 8 + f8) * 2;
        *(unsigned short*)(d) = f2bf(rbf(gelu_tanh_f(g0)) * u0);
        *(unsigned short*)(d + EP_LD) = f2bf(rbf(gelu_tanh_f(g1)) * u1);
      }
    } else {
      const int p = P0 + jj * 8 - FF + f8;
      const float2 cf = coef[p];
      const float2 lam = lamb[p];
      const float l2r = lam.x * lam.x - lam.y * lam.y, l2i = 2.f * lam.x * lam.y;
      const float l4r = l2r * l2r - l2i * l2i, l4i = 2.f * l2r * l2i;
      const float l8r = l4r * l4r - l4i * l4i, l8i = 2.f * l4r * l4i;
      const float l16r = l8r * l8r - l8i * l8i, l16i = 2.f * l8r * l8i;
      float mr = (fq2 & 1) ? 1.f : l4r, mim = (fq2 & 1) ? 0.f : l4i;
      if (!(fq2 & 2)) { const float a_ = mr * l8r - mim * l8i, b_ = mr * l8i + mim * l8r; mr = a_; mim = b_; }
      if (!hi) { const float a_ = mr * l2r - mim * l2i, b_ = mr * l2i + mim * l2r; mr = a_; mim = b_; }
      float er[4], ei[4];
#pragma unroll
      for (int mi = 0; mi < 4; ++mi) {
        const f32x4 v = acc[mi][jj];
        const float r0 = __shfl_xor(hi ? v[0] : v[2], 8), r1 = __shfl_xor(hi ? v[1] : v[3], 8);
        const float ur0 = hi ? r0 : v[0], ur1 = hi ? r1 : v[1], ui0 = hi ? v[2] : r0, ui1 = hi ? v[3] : r1;
        const float b0r = cf.x * ur0 - cf.y * ui0, b0i = cf.x * ui0 + cf.y * ur0;
        const float b1r = cf.x * ur1 - cf.y * ui1, b1i = cf.x * ui1 + cf.y * ur1;
        const int row = brow + wr * 64 + mi * 16 + rb;
        bu[(size_t)row * PP + p] = make_float2(b0r, b0i);
        bu[(size_t)(row + 1) * PP + p] = make_float2(b1r, b1i);
        er[mi] = lam.x * b0r - lam.y * b0i + b1r; ei[mi] = lam.x * b0i + lam.y * b0r + b1i;
      }
#pragma unroll
      for (int ch = 0; ch < 2; ++ch) {
        const float tr_ = l16r * er[2 * ch] - l16i * ei[2 * ch] + er[2 * ch + 1], ti_ = l16r * ei[2 * ch] + l16i * er[2 * ch] + ei[2 * ch + 1];
        float sr = mr * tr_ - mim * ti_, si = mr * ti_ + mim * tr_;
        sr += __shfl_xor(sr, 8); si += __shfl_xor(si, 8);
        sr += __shfl_xor(sr, 16); si += __shfl_xor(si, 16);
        sr += __shfl_xor(sr, 32); si += __shfl_xor(si, 32);
        if (lane2 < 8) Eo[(size_t)(((brow + wr * 64) >> 5) + ch) * PP + p] = make_float2(sr, si);
      }
    }
  }
  asm volatile("s_waitcnt lgkmcnt(0)" ::: "memory");
  {
    unsigned short* dst0 = ah + (size_t)(brow + wr * 64) * KAH + P0;
#pragma unroll
    for (int i = 0; i < 9; ++i) {
      const int q = i * 64 + lane2, row = q / 9, ch = q % 9;
      if (ch < nf) {
        bf16x8 v = *(const bf16x8*)(wl + row * EP_LD + ch * 16);
        *(bf16x8*)(dst0 + (size_t)row * KAH + ch * 8) = v;
      }
    }
  }
}

__global__ __launch_bounds__(256) void scanA_kernel(char* __restrict__ ws) {
  const int p = (blockIdx.x & 3) * 256 + threadIdx.x, c = blockIdx.x >> 2;
  const float2 lam = ((const float2*)(ws + OFF_LAM))[p];
  const float2* bu = (const float2*)(ws + OFF_BU) + (size_t)c * SCAN_T * PP + p;
  float er = 0.f, ei = 0.f;
#pragma unroll 8
  for (int s = 0; s < SCAN_T; ++s) {
    float2 b = bu[(size_t)s * PP];
    float nr = lam.x * er - lam.y * ei + b.x, ni = lam.x * ei + lam.y * er + b.y;
    er = nr; ei = ni;
  }
  ((float2*)(ws + OFF_E))[(size_t)c * PP + p] = make_float2(er, ei);
}

__global__ __launch_bounds__(256) void scanC_kernel(char* __restrict__ ws) {
  const int p = (blockIdx.x & 3) * 256 + threadIdx.x, c = blockIdx.x >> 2;
  const float2* bu = (const float2*)(ws + OFF_BU) + (size_t)c * SCAN_T * PP + p;
  float2 b[SCAN_T];
#pragma unroll
  for (int s = 0; s < SCAN_T; ++s) b[s] = bu[(size_t)s * PP];
  const float2 lam = ((const float2*)(ws + OFF_LAM))[p];
  const float2* E = (const float2*)(ws + OFF_E) + p;
  float lr = lam.x, li = lam.y;
#pragma unroll
  for (int i = 0; i < 5; ++i) { float a = lr * lr - li * li, bb = 2.f * lr * li; lr = a; li = bb; }
  float hr = 0.f, hi = 0.f;
  {
    float2 e[NCHUNK - 1];
#pragma unroll
    for (int u = 0; u < NCHUNK - 1; ++u) e[u] = (u < c) ? E[(size_t)u * PP] : make_float2(0.f, 0.f);
#pragma unroll
    for (int u = 0; u < NCHUNK - 1; ++u) if (u < c) { float nr = lr * hr - li * hi + e[u].x, ni = lr * hi + li * hr + e[u].y; hr = nr; hi = ni; }
  }
  unsigned short* ah = (unsigned short*)(ws + OFF_AH) + (size_t)c * SCAN_T * KAH + FF + p;
#pragma unroll
  for (int s = 0; s < SCAN_T; ++s) {
    float nr = lam.x * hr - lam.y * hi + b[s].x, ni = lam.x * hi + lam.y * hr + b[s].y;
    hr = nr; hi = ni;
    ah[(size_t)s * KAH] = f2bf(hr);
    ah[(size_t)s * KAH + PP] = f2bf(hi);
  }
}

constexpr int G2_BM = 128, G2_BN = 64, G2_CH = G2_BN / 8;
constexpr int G2_A_BYTES = G2_BM * 64 * 2, G2_B_BYTES = 64 * G2_BN * 2, G2_STAGE = G2_A_BYTES + G2_B_BYTES;
constexpr int G2_NST = 5;
__device__ __forceinline__ int g2_swz(int k) { return (((k >> 1) & 1) << 1) | (((k >> 3) & 1) << 2); }

__global__ __launch_bounds__(256) void gemm2_kernel(
    const bf16* __restrict__ x, const bf16* __restrict__ wd, const bf16* __restrict__ dskip,
    const char* __restrict__ ws, float* __restrict__ out) {
  extern __shared__ __attribute__((aligned(16))) char smem[];
  const int tid = threadIdx.x, lane = tid & 63, wid = __builtin_amdgcn_readfirstlane(tid >> 6);
  const int bid = blockIdx.x;
  const int xcd = bid & 7, idx = bid >> 3;
  const int pm = (xcd & 3) * 4 + (idx & 3), pn = (xcd >> 2) * 8 + (idx >> 2);
  const int brow = pm * G2_BM, bcol = pn * G2_BN;
  const unsigned short* ah = (const unsigned short*)(ws + OFF_AH);
  const unsigned short* ct = (const unsigned short*)(ws + OFF_CT);
  const unsigned short* asrc[4];
#pragma unroll
  for (int i = 0; i < 4; ++i) {
    int R = (i * 4 + wid) * 8 + (lane >> 3), C = ((lane & 7) ^ (R & 7)) * 8;
    asrc[i] = ah + (size_t)(brow + R) * KAH + C;
  }
  int boffs[2];
#pragma unroll
  for (int i = 0; i < 2; ++i) {
    int Lc = (i * 4 + wid) * 64 + lane, k = Lc / G2_CH, cp = Lc % G2_CH, c = cp ^ g2_swz(k);
    boffs[i] = k * DM + bcol + c * 8;
  }
#define ABL_A(x) x
#define ABL_B(x) x
#define G2_STAGE_LOADS(buf, kt) do { \
    char* sa_ = smem + (buf) * G2_STAGE; char* sb_ = sa_ + G2_A_BYTES; \
    ABL_A(_Pragma("unroll") for (int i = 0; i < 4; ++i) GLDS16(asrc[i] + (kt) * 64, sa_ + (i * 4 + wid) * 1024);) \
    const unsigned short* bb_ = (kt) < 56 ? (const unsigned short*)wd + (size_t)(kt) * 64 * DM : ct + (size_t)((kt) - 56) * 64 * DM; \
    ABL_B(_Pragma("unroll") for (int i = 0; i < 2; ++i) GLDS16(bb_ + boffs[i], sb_ + (i * 4 + wid) * 1024);) \
  } while (0)
  const int wr = wid >> 1, wc = wid & 1;
  const int fr = lane & 15, fq = lane >> 4, tq = (lane & 15) >> 2, tp = lane & 3;
  f32x4 accd[4][2], accy[4][2];
#pragma unroll
  for (int i = 0; i < 4; ++i)
#pragma unroll
    for (int j = 0; j < 2; ++j) { accd[i][j] = (f32x4){0.f, 0.f, 0.f, 0.f}; accy[i][j] = (f32x4){0.f, 0.f, 0.f, 0.f}; }

  const unsigned ldsb = (unsigned)(size_t)(__attribute__((address_space(3))) char*)smem;
  const unsigned aA0 = ldsb + (wr * 64 + fr) * 128 + ((fq ^ (fr & 7)) * 16);
  const unsigned aA1 = ldsb + (wr * 64 + fr) * 128 + (((4 + fq) ^ (fr & 7)) * 16);
  unsigned aB[2][2];
#pragma unroll
  for (int nj = 0; nj < 2; ++nj)
#pragma unroll
    for (int t = 0; t < 2; ++t) {
      const int k0 = 8 * fq + tq + 4 * t, j = wc * 2 + nj;
      aB[nj][t] = ldsb + G2_A_BYTES + (k0 * G2_CH + ((2 * j + (tp >> 1)) ^ g2_swz(k0))) * 16 + 8 * (tp & 1);
    }
#define G2_RD(af, bfm, buf, kk) do { \
    bfm[0] = join_frag(lds_tr64<(kk) * 32 * G2_CH * 16>(aB[0][0] + (buf) * G2_STAGE), lds_tr64<(kk) * 32 * G2_CH * 16>(aB[0][1] + (buf) * G2_STAGE)); \
    bfm[1] = join_frag(lds_tr64<(kk) * 32 * G2_CH * 16>(aB[1][0] + (buf) * G2_STAGE), lds_tr64<(kk) * 32 * G2_CH * 16>(aB[1][1] + (buf) * G2_STAGE)); \
    af[0] = lds_b128<0>(((kk) ? aA1 : aA0) + (buf) * G2_STAGE); af[1] = lds_b128<2048>(((kk) ? aA1 : aA0) + (buf) * G2_STAGE); \
    af[2] = lds_b128<4096>(((kk) ? aA1 : aA0) + (buf) * G2_STAGE); af[3] = lds_b128<6144>(((kk) ? aA1 : aA0) + (buf) * G2_STAGE); \
  } while (0)
#define G2_MMA(acc, af, bfm) do { \
    _Pragma("unroll") for (int mi = 0; mi < 4; ++mi) \
      _Pragma("unroll") for (int nj = 0; nj < 2; ++nj) acc[mi][nj] = __builtin_amdgcn_mfma_f32_16x16x32_bf16(af[mi], bfm[nj], acc[mi][nj], 0, 0, 0); \
  } while (0)
#define compute(buf, acc) do { \
    bf16x8 a0[4], a1[4], f0[2], f1[2]; \
    G2_RD(a0, f0, buf, 0); \
    WAIT_LGKM(0); \
    G2_RD(a1, f1, buf, 1); \
    G2_MMA(acc, a0, f0); \
    WAIT_LGKM(0); \
    G2_MMA(acc, a1, f1); \
  } while (0)
  G2_STAGE_LOADS(0, 0); G2_STAGE_LOADS(1, 1); G2_STAGE_LOADS(2, 2); G2_STAGE_LOADS(3, 3);
#define G2_STEP(u, kt, acc) do { WAIT_VM(18); RAW_BAR(); G2_STAGE_LOADS(((u) + 4) % 5, (kt) + 4); compute(u, acc); } while (0)
  for (int kt0 = 0; kt0 < 55; kt0 += 5) {
    G2_STEP(0, kt0, accd); G2_STEP(1, kt0 + 1, accd); G2_STEP(2, kt0 + 2, accd); G2_STEP(3, kt0 + 3, accd); G2_STEP(4, kt0 + 4, accd);
  }
  WAIT_VM(18); RAW_BAR(); G2_STAGE_LOADS(4, 59); compute(0, accd);
  for (int kt0 = 56; kt0 < 81; kt0 += 5) {
    G2_STEP(1, kt0, accy); G2_STEP(2, kt0 + 1, accy); G2_STEP(3, kt0 + 2, accy); G2_STEP(4, kt0 + 3, accy); G2_STEP(0, kt0 + 4, accy);
  }
  WAIT_VM(18); RAW_BAR(); G2_STAGE_LOADS(0, 85); compute(1, accy);
  WAIT_VM(18); RAW_BAR(); G2_STAGE_LOADS(1, 86); compute(2, accy);
  WAIT_VM(18); RAW_BAR(); G2_STAGE_LOADS(2, 87); compute(3, accy);
  WAIT_VM(18); RAW_BAR(); compute(4, accy);
  WAIT_VM(12); RAW_BAR(); compute(0, accy);
  WAIT_VM(6); RAW_BAR(); compute(1, accy);
  WAIT_VM(0); RAW_BAR(); compute(2, accy);
  const unsigned short* xs = (const unsigned short*)x;
  const unsigned short* xn = (const unsigned short*)(ws + OFF_XN);
  WAIT_VM(0); RAW_BAR();
  constexpr int EP_LD = 32 * 8 + 16;
  char* wl = smem + wid * (64 * EP_LD);
#pragma unroll
  for (int nj = 0; nj < 2; ++nj)
#pragma unroll
    for (int mi = 0; mi < 4; ++mi)
#pragma unroll
      for (int r = 0; r < 4; ++r) {
        const float dn = rbf(accd[mi][nj][r]);
        const float g = rbf(dn * __builtin_amdgcn_rcpf(1.0f + __expf(-dn)));
        *(float2*)(wl + (mi * 16 + fq * 4 + r) * EP_LD + (nj * 16 + fr) * 8) = make_float2(2.0f * accy[mi][nj][r], g);
      }
  asm volatile("s_waitcnt lgkmcnt(0)" ::: "memory");
  {
    const int c4 = (lane & 7) * 4, rsub = lane >> 3;
    const int col = bcol + wc * 32 + c4;
    const unsigned short* dk = (const unsigned short*)dskip + col;
    const bf16x4 dk4 = *(const bf16x4*)dk;
#pragma unroll
    for (int i = 0; i < 8; ++i) {
      const int rl = i * 8 + rsub, row = brow + wr * 64 + rl;
      const size_t o = (size_t)row * DM + col;
      const bf16x4 x4 = *(const bf16x4*)(xs + o), n4 = *(const bf16x4*)(xn + (size_t)row * LDXN + col);
      const f32x4 p0 = *(const f32x4*)(wl + rl * EP_LD + c4 * 8), p1 = *(const f32x4*)(wl + rl * EP_LD + c4 * 8 + 16);
      f32x4 res;
      res[0] = bf2f((unsigned short)x4[0]) + (p0[0] + rbf(bf2f((unsigned short)n4[0]) * bf2f((unsigned short)dk4[0]))) * p0[1];
      res[1] = bf2f((unsigned short)x4[1]) + (p0[2] + rbf(bf2f((unsigned short)n4[1]) * bf2f((unsigned short)dk4[1]))) * p0[3];
      res[2] = bf2f((unsigned short)x4[2]) + (p1[0] + rbf(bf2f((unsigned short)n4[2]) * bf2f((unsigned short)dk4[2]))) * p1[1];
      res[3] = bf2f((unsigned short)x4[3]) + (p1[2] + rbf(bf2f((unsigned short)n4[3]) * bf2f((unsigned short)dk4[3]))) * p1[3];
      *(f32x4*)(out + o) = res;
    }
  }
}

extern "C" void kernel_launch(void* const* d_in, const int* in_sizes, int n_in,
                              void* d_out, int out_size, void* d_ws, size_t ws_size,
                              hipStream_t stream) {
  const bf16* x = (const bf16*)d_in[0];
  const bf16* lam_re = (const bf16*)d_in[1];
  const bf16* lam_im = (const bf16*)d_in[2];
  const bf16* b_re = (const bf16*)d_in[3];
  const bf16* b_im = (const bf16*)d_in[4];
  const bf16* c_re = (const bf16*)d_in[5];
  const bf16* c_im = (const bf16*)d_in[6];
  const bf16* log_dt = (const bf16*)d_in[7];
  const bf16* dskip = (const bf16*)d_in[8];
  const bf16* wg = (const bf16*)d_in[10];
  const bf16* wu = (const bf16*)d_in[11];
  const bf16* wd = (const bf16*)d_in[12];
  char* ws = (char*)d_ws;
  float* out = (float*)d_out;

  hipFuncSetAttribute((const void*)gemm1_kernel, hipFuncAttributeMaxDynamicSharedMemorySize, G1_NST * G1_STAGE);
  hipFuncSetAttribute((const void*)gemm2_kernel, hipFuncAttributeMaxDynamicSharedMemorySize, G2_NST * G2_STAGE);

  prep_kernel<<<PREP_BLOCKS, 256, 0, stream>>>(x, lam_re, lam_im, b_re, b_im, c_re, c_im, log_dt, ws);
  gemm1_kernel<<<256, 512, G1_NST * G1_STAGE, stream>>>(wg, wu, ws);
  scanC_kernel<<<4 * NCHUNK, 256, 0, stream>>>(ws);
  gemm2_kernel<<<256, 256, G2_NST * G2_STAGE, stream>>>(x, wd, dskip, ws, out);
}
```

```cpp
#include <hip/hip_runtime.h>
#include <hip/hip_bf16.h>
#include <stdint.h>

typedef __hip_bfloat16 bf16;
typedef __attribute__((ext_vector_type(8))) short bf16x8;
typedef __attribute__((ext_vector_type(4))) short bf16x4;
typedef __attribute__((ext_vector_type(4))) float f32x4;
typedef __attribute__((__vector_size__(4 * sizeof(short)))) short v4s;

constexpr int L_SEQ = 2048, DM = 1024, FF = 3584, PP = 1024;
constexpr int LDW = 3584;
constexpr int LDXN = DM + 64;
constexpr int KAH = FF + 2 * PP + 64;
constexpr int SCAN_T = 32, NCHUNK = L_SEQ / SCAN_T;

constexpr size_t OFF_XN   = 0;
constexpr size_t OFF_BT   = OFF_XN + (size_t)L_SEQ * LDXN * 2;
constexpr size_t OFF_CT   = OFF_BT + (size_t)DM * LDW * 2;
constexpr size_t OFF_AH   = OFF_CT + (size_t)2 * PP * DM * 2;
constexpr size_t OFF_BU   = OFF_AH + (size_t)L_SEQ * KAH * 2;
constexpr size_t OFF_E    = OFF_BU + (size_t)L_SEQ * PP * 8;
constexpr size_t OFF_LAM  = OFF_E + (size_t)NCHUNK * PP * 8;
constexpr size_t OFF_COEF = OFF_LAM + (size_t)PP * 8;

__device__ __forceinline__ float bf2f(unsigned short u) { return __uint_as_float(((unsigned)u) << 16); }
__device__ __forceinline__ unsigned short f2bf(float f) {
  __bf16 b = (__bf16)f;
  return __builtin_bit_cast(unsigned short, b);
}
__device__ __forceinline__ float rbf(float f) { return bf2f(f2bf(f)); }

constexpr int PREP_LN_BLOCKS = L_SEQ / 4;
constexpr int PREP_TR_BLOCKS = 4 * (1024 / 64) * (1024 / 64);
constexpr int PREP_BLOCKS = PREP_LN_BLOCKS + PREP_TR_BLOCKS + 4;

__global__ __launch_bounds__(256) void prep_kernel(
    const bf16* __restrict__ x, const bf16* __restrict__ lam_re, const bf16* __restrict__ lam_im,
    const bf16* __restrict__ b_re, const bf16* __restrict__ b_im,
    const bf16* __restrict__ c_re, const bf16* __restrict__ c_im,
    const bf16* __restrict__ log_dt, char* __restrict__ ws) {
  __shared__ unsigned short tile[64][66];
  const int bid = blockIdx.x, tid = threadIdx.x;
  if (bid < PREP_LN_BLOCKS) {
    const int wave = tid >> 6, lane = tid & 63;
    const int row = bid * 4 + wave;
    const unsigned short* xr = (const unsigned short*)x + (size_t)row * DM;
    float v[16];
    bf16x8 a = *(const bf16x8*)(xr + lane * 8);
    bf16x8 b = *(const bf16x8*)(xr + 512 + lane * 8);
    float sum = 0.f;
#pragma unroll
    for (int j = 0; j < 8; ++j) { v[j] = bf2f((unsigned short)a[j]); v[8 + j] = bf2f((unsigned short)b[j]); }
#pragma unroll
    for (int j = 0; j < 16; ++j) sum += v[j];
#pragma unroll
    for (int o = 32; o > 0; o >>= 1) sum += __shfl_xor(sum, o);
    const float mean = sum * (1.0f / DM);
    float sq = 0.f;
#pragma unroll
    for (int j = 0; j < 16; ++j) { float d = v[j] - mean; sq += d * d; }
#pragma unroll
    for (int o = 32; o > 0; o >>= 1) sq += __shfl_xor(sq, o);
    const float var_b = rbf(sq * (1.0f / DM));
    const float mean_b = rbf(mean);
    const float den = rbf(sqrtf(rbf(var_b + 1e-5f)));
    bf16x8 oa, ob;
#pragma unroll
    for (int j = 0; j < 8; ++j) {
      oa[j] = (short)f2bf(rbf(v[j] - mean_b) / den);
      ob[j] = (short)f2bf(rbf(v[8 + j] - mean_b) / den);
    }
    unsigned short* xo = (unsigned short*)(ws + OFF_XN) + (size_t)row * LDXN;
    *(bf16x8*)(xo + lane * 8) = oa;
    *(bf16x8*)(xo + 512 + lane * 8) = ob;
  } else if (bid < PREP_LN_BLOCKS + PREP_TR_BLOCKS) {
    const int t = bid - PREP_LN_BLOCKS;
    const int mat = t >> 8, ti = (t >> 4) & 15, tj = t & 15;
    const unsigned short* src = (const unsigned short*)(mat == 0 ? b_re : mat == 1 ? b_im : mat == 2 ? c_re : c_im);
    const int tc = tid & 7, tr = tid >> 3;
#pragma unroll
    for (int i = 0; i < 2; ++i) {
      const int r = tr + 32 * i;
      bf16x8 v = *(const bf16x8*)(src + (size_t)(ti * 64 + r) * 1024 + tj * 64 + tc * 8);
#pragma unroll
      for (int e = 0; e < 8; ++e) tile[r][tc * 8 + e] = (unsigned short)v[e];
    }
    __syncthreads();
    unsigned short* dst; size_t ld; unsigned short neg = 0;
    if (mat < 2) { dst = (unsigned short*)(ws + OFF_BT) + mat * 1024; ld = LDW; }
    else { dst = (unsigned short*)(ws + OFF_CT) + (size_t)(mat - 2) * 1024 * DM; ld = DM; neg = (mat == 3) ? 0x8000 : 0; }
#pragma unroll
    for (int i = 0; i < 2; ++i) {
      const int c = tr + 32 * i;
      bf16x8 v;
#pragma unroll
      for (int e = 0; e < 8; ++e) v[e] = (short)(tile[tc * 8 + e][c] ^ neg);
      *(bf16x8*)(dst + (size_t)(tj * 64 + c) * ld + ti * 64 + tc * 8) = v;
    }
  } else {
    const int p = (bid - PREP_LN_BLOCKS - PREP_TR_BLOCKS) * 256 + tid;
    const float lr = bf2f(((const unsigned short*)lam_re)[p]);
    const float li = bf2f(((const unsigned short*)lam_im)[p]);
    const float dt = rbf(expf(bf2f(((const unsigned short*)log_dt)[p])));
    const float ar = dt * lr, ai = dt * li;
    const float e = expf(ar);
    float sn, cs; sincosf(ai, &sn, &cs);
    const float br_ = e * cs, bi_ = e * sn;
    const float nr = br_ - 1.0f, ni = bi_;
    const float den = lr * lr + li * li;
    const float cr = (nr * lr + ni * li) / den, ci = (ni * lr - nr * li) / den;
    ((float2*)(ws + OFF_LAM))[p] = make_float2(br_, bi_);
    ((float2*)(ws + OFF_COEF))[p] = make_float2(cr, ci);
  }
}

__device__ __forceinline__ int a_lds_byte(int r, int c) {
  int st = (r >> 4) * 2 + (c >> 5), rr = r & 15, cc = c & 31, ob = rr * 64 + cc * 2;
  return st * 1024 + (ob ^ (((ob >> 9) & 1) << 5));
}
__device__ __forceinline__ void a_stage_rc(int b, int& R, int& C) {
  int st = b >> 10, sb = b & 1023, swz = sb ^ (((sb >> 9) & 1) << 5);
  R = (st >> 1) * 16 + (swz >> 6); C = (st & 1) * 32 + ((swz & 63) >> 1);
}
__device__ __forceinline__ bf16x8 tr_frag(const char* lds_lo, const char* lds_hi) {
  v4s lo = __builtin_amdgcn_ds_read_tr16_b64_v4i16((__attribute__((address_space(3))) v4s*)(lds_lo));
  v4s hi = __builtin_amdgcn_ds_read_tr16_b64_v4i16((__attribute__((address_space(3))) v4s*)(lds_hi));
  bf16x8 r;
  r[0] = lo[0]; r[1] = lo[1]; r[2] = lo[2]; r[3] = lo[3];
  r[4] = hi[0]; r[5] = hi[1]; r[6] = hi[2]; r[7] = hi[3];
  return r;
}
typedef __attribute__((ext_vector_type(2))) unsigned u32x2;
typedef __attribute__((ext_vector_type(4))) unsigned u32x4;
template <int OFF> __device__ __forceinline__ u32x2 lds_tr64(unsigned addr) {
  u32x2 r; asm volatile("ds_read_b64_tr_b16 %0, %1 offset:%2" : "=v"(r) : "v"(addr), "i"(OFF) : "memory"); return r;
}
template <int OFF> __device__ __forceinline__ bf16x8 lds_b128(unsigned addr) {
  u32x4 r; asm volatile("ds_read_b128 %0, %1 offset:%2" : "=v"(r) : "v"(addr), "i"(OFF) : "memory"); return __builtin_bit_cast(bf16x8, r);
}
__device__ __forceinline__ bf16x8 join_frag(u32x2 lo, u32x2 hi) {
  u32x4 r = __builtin_shufflevector(lo, hi, 0, 1, 2, 3); return __builtin_bit_cast(bf16x8, r);
}
#define WAIT_LGKM(n) do { asm volatile("s_waitcnt lgkmcnt(" #n ")" ::: "memory"); __builtin_amdgcn_sched_barrier(0); } while (0)
#define GLDS16(gptr, ldsptr) __builtin_amdgcn_global_load_lds((const void __attribute__((address_space(1)))*)(gptr), (void __attribute__((address_space(3)))*)(ldsptr), 16, 0, 0)

__device__ __forceinline__ float gelu_tanh_f(float x) {
  const float k0 = 0.7978845608028654f, k1 = 0.044715f;
  float u = k0 * (x + k1 * x * x * x);
  return x * __builtin_amdgcn_rcpf(1.0f + __expf(-2.0f * u));
}

constexpr int G1_BM = 256, G1_BN = 288, G1_CH = G1_BN / 8;
constexpr int G1_BK = 32, G1_NST = 4, G1_NK = 1024 / G1_BK;
constexpr int G1_A_BYTES = G1_BM * G1_BK * 2, G1_B_BYTES = G1_BK * G1_BN * 2, G1_STAGE = G1_A_BYTES + G1_B_BYTES;
__device__ __forceinline__ int g1_swz(int k) { return ((k >> 3) & 1) << 1; }
#define WAIT_VM(n) asm volatile("s_waitcnt vmcnt(" #n ") lgkmcnt(0)" ::: "memory")
#define RAW_BAR() __builtin_amdgcn_s_barrier()

__global__ __launch_bounds__(512, 2) void gemm1_kernel(
    const bf16* __restrict__ wg, const bf16* __restrict__ wu, char* __restrict__ ws) {
  extern __shared__ __attribute__((aligned(16))) char smem[];
  const int tid = threadIdx.x, lane = tid & 63, wid = __builtin_amdgcn_readfirstlane(tid >> 6);
  const int bid = blockIdx.x;
  const int xcd = bid & 7, idx = bid >> 3;
  const int pn = xcd * 4 + (idx & 3), pm = idx >> 2;
  const int brow = pm * G1_BM;
  const int wr = wid & 3, wc = wid >> 2;
  const unsigned short* xn = (const unsigned short*)(ws + OFF_XN);
  const unsigned short* bt = (const unsigned short*)(ws + OFF_BT);

  unsigned aoffs[2];
#pragma unroll
  for (int i = 0; i < 2; ++i) {
    int sb = lane * 16, swz = sb ^ (((sb >> 9) & 1) << 5);
    int R = (i * 8 + wid) * 16 + (swz >> 6), C = (swz & 63) >> 1;
    aoffs[i] = (unsigned)((brow + R) * LDXN + C);
  }
  const unsigned short* bsrc[3];
#pragma unroll
  for (int i = 0; i < 3; ++i) {
    int inst = i * 8 + wid; if (inst > 17) inst = 17;
    int Lc = inst * 64 + lane, k = Lc / G1_CH, cp = Lc % G1_CH, c = cp ^ g1_swz(k);
    int half = c & 1, P = pn * 144 + (c >> 1) * 8;
    const unsigned short* base = (const unsigned short*)(half ? wu : wg);
    int o = P;
    if (P >= FF) { base = bt; o = half * 1024 + (P - FF); }
    bsrc[i] = base + (k * LDW + o);
  }
#define G1_STAGE_LOADS(buf) do { \
    char* sa_ = smem + (buf) * G1_STAGE; char* sb_ = sa_ + G1_A_BYTES; \
    _Pragma("unroll") for (int i = 0; i < 2; ++i) { GLDS16(xn + aoffs[i], sa_ + (i * 8 + wid) * 1024); aoffs[i] += G1_BK; } \
    _Pragma("unroll") for (int i = 0; i < 2; ++i) { GLDS16(bsrc[i], sb_ + (i * 8 + wid) * 1024); bsrc[i] += G1_BK * LDW; } \
    if (wid < 2) { GLDS16(bsrc[2], sb_ + (16 + wid) * 1024); bsrc[2] += G1_BK * LDW; } \
  } while (0)
  f32x4 acc[4][9];
#pragma unroll
  for (int i = 0; i < 4; ++i)
#pragma unroll
    for (int j = 0; j < 9; ++j) acc[i][j] = (f32x4){0.f, 0.f, 0.f, 0.f};

  const int fr = lane & 15, fq = lane >> 4, tq = (lane & 15) >> 2, tp = lane & 3;
  const int ob_ = fr * 64 + fq * 16, aoff0 = (wr * 4) * 1024 + (ob_ ^ (((ob_ >> 9) & 1) << 5));
  const int bRow = G1_A_BYTES + ((8 * fq + tq) * G1_CH + (tp >> 1)) * 16 + 8 * (tp & 1) + 32 * 9 * wc;
  const int sgn = 32 * (fq & 1);
  const unsigned ldsb = (unsigned)(size_t)(__attribute__((address_space(3))) char*)smem;
  const unsigned aA = ldsb + aoff0;
  const unsigned aE = ldsb + bRow + (wc ? -sgn : sgn);
  const unsigned aO = ldsb + bRow + (wc ? sgn : -sgn);
#define G1_RDB1(jj, ad) join_frag(lds_tr64<32 * (jj)>((ad) + bo_), lds_tr64<32 * (jj) + 4 * G1_CH * 16>((ad) + bo_))
#define G1_READ(buf) do { \
    const unsigned bo_ = (buf) * G1_STAGE; \
    bfr[0] = G1_RDB1(0, aE); bfr[1] = G1_RDB1(1, aO); bfr[2] = G1_RDB1(2, aE); \
    af[0] = lds_b128<0>(aA + bo_); af[1] = lds_b128<1024>(aA + bo_); af[2] = lds_b128<2048>(aA + bo_); af[3] = lds_b128<3072>(aA + bo_); \
    bfr[3] = G1_RDB1(3, aO); bfr[4] = G1_RDB1(4, aE); bfr[5] = G1_RDB1(5, aO); \
    bfr[6] = G1_RDB1(6, aE); bfr[7] = G1_RDB1(7, aO); bfr[8] = G1_RDB1(8, aE); \
  } while (0)
#define G1_MMA() do { \
    __builtin_amdgcn_s_setprio(1); \
    _Pragma("unroll") for (int jj = 0; jj < 9; ++jj) \
      _Pragma("unroll") for (int mi = 0; mi < 4; ++mi) \
        acc[mi][jj] = __builtin_amdgcn_mfma_f32_16x16x32_bf16(af[mi], bfr[jj], acc[mi][jj], 0, 0, 0); \
    __builtin_amdgcn_s_setprio(0); \
  } while (0)
#define G1_WAITN(n2, n1) do { if (wid < 2) WAIT_VM(n2); else WAIT_VM(n1); __builtin_amdgcn_sched_barrier(0); } while (0)
#define G1_STEP(u, LOADS, w2, w1) do { \
    G1_READ(u); \
    LOADS; \
    G1_WAITN(w2, w1); RAW_BAR(); \
    G1_MMA(); \
    G1_WAITN(w2, w1); RAW_BAR(); \
  } while (0)

  bf16x8 af[4], bfr[9];
  G1_STAGE_LOADS(0); G1_STAGE_LOADS(1); G1_STAGE_LOADS(2);
  G1_WAITN(10, 8); RAW_BAR();
  if (wc == 1) RAW_BAR();
  for (int kt0 = 0; kt0 < G1_NK - 4; kt0 += 4) {
    G1_STEP(0, G1_STAGE_LOADS(3), 10, 8);
    G1_STEP(1, G1_STAGE_LOADS(0), 10, 8);
    G1_STEP(2, G1_STAGE_LOADS(1), 10, 8);
    G1_STEP(3, G1_STAGE_LOADS(2), 10, 8);
  }
  G1_STEP(0, G1_STAGE_LOADS(3), 10, 8);
  G1_STEP(1, , 5, 4);
  G1_STEP(2, , 0, 0);
  G1_STEP(3, , 0, 0);
  if (wc == 0) RAW_BAR();

  int tz = threadIdx.x; asm volatile("" : "+v"(tz));
  const int lane2 = tz & 63, fr2 = tz & 15, fq2 = (tz >> 4) & 3;
  const bool hi = fr2 >= 8;
  const int f8 = fr2 & 7, rb = fq2 * 4 + (hi ? 2 : 0);
  unsigned short* ah = (unsigned short*)(ws + OFF_AH);
  float2* bu = (float2*)(ws + OFF_BU);
  const float2* coef = (const float2*)(ws + OFF_COEF);
  const float2* lamb = (const float2*)(ws + OFF_LAM);
  float2* Eo = (float2*)(ws + OFF_E);
  const int P0 = pn * 144 + wc * 72;
  const int nf = min(9, max(0, (FF - P0) >> 3));
  RAW_BAR();
  constexpr int EP_LD = 160;
  char* wl = smem + wid * (64 * EP_LD);
  if (nf > 0) {
    unsigned short* dst0 = ah + (size_t)(brow + wr * 64) * KAH + P0;
#pragma unroll
    for (int mi = 0; mi < 4; ++mi) {
#pragma unroll
      for (int jj = 0; jj < 9; ++jj) {
        if (jj < nf) {
          const f32x4 v = acc[mi][jj];
          const float r0 = __shfl_xor(hi ? v[0] : v[2], 8), r1 = __shfl_xor(hi ? v[1] : v[3], 8);
          const float g0 = rbf(hi ? r0 : v[0]), g1 = rbf(hi ? r1 : v[1]);
          const float u0 = rbf(hi ? v[2] : r0), u1 = rbf(hi ? v[3] : r1);
          char* d = wl + (mi * 16 + rb) * EP_LD + (jj * 8 + f8) * 2;
          *(unsigned short*)(d) = f2bf(rbf(gelu_tanh_f(g0)) * u0);
          *(unsigned short*)(d + EP_LD) = f2bf(rbf(gelu_tanh_f(g1)) * u1);
        }
      }
      asm volatile("s_waitcnt lgkmcnt(0)" ::: "memory");
#pragma unroll
      for (int i = 0; i < 3; ++i) {
        const int q = i * 64 + lane2, row = mi * 16 + q / 9, ch = q % 9;
        if (q < 144 && ch < nf) {
          bf16x8 v = *(const bf16x8*)(wl + row * EP_LD + ch * 16);
          *(bf16x8*)(dst0 + (size_t)row * KAH + ch * 8) = v;
        }
      }
    }
  }
  if (nf < 9) {
    constexpr int EB_LD = 72 * 8 + 16;
    char* wb = smem + wid * (64 * EP_LD);
    const int pbase = P0 - FF;
    float2 cf[9], lm[9];
    float ger[9], gei[9];
#pragma unroll
    for (int jj = 0; jj < 9; ++jj) if (jj >= nf) { cf[jj] = coef[pbase + jj * 8 + f8]; lm[jj] = lamb[pbase + jj * 8 + f8]; }
#pragma unroll
    for (int mi = 0; mi < 4; ++mi) {
      asm volatile("s_waitcnt lgkmcnt(0)" ::: "memory");
#pragma unroll
      for (int jj = 0; jj < 9; ++jj) {
        if (jj >= nf) {
          const f32x4 v = acc[mi][jj];
          const float2 c_ = cf[jj], lam = lm[jj];
          const float r0 = __shfl_xor(hi ? v[0] : v[2], 8), r1 = __shfl_xor(hi ? v[1] : v[3], 8);
          const float ur0 = hi ? r0 : v[0], ur1 = hi ? r1 : v[1], ui0 = hi ? v[2] : r0, ui1 = hi ? v[3] : r1;
          const float b0r = c_.x * ur0 - c_.y * ui0, b0i = c_.x * ui0 + c_.y * ur0;
          const float b1r = c_.x * ur1 - c_.y * ui1, b1i = c_.x * ui1 + c_.y * ur1;
          *(float2*)(wb + rb * EB_LD + (jj * 8 + f8) * 8) = make_float2(b0r, b0i);
          *(float2*)(wb + (rb + 1) * EB_LD + (jj * 8 + f8) * 8) = make_float2(b1r, b1i);
          const float er = lam.x * b0r - lam.y * b0i + b1r, ei = lam.x * b0i + lam.y * b0r + b1i;
          if ((mi & 1) == 0) { ger[jj] = er; gei[jj] = ei; }
          else {
            const float l2r = lam.x * lam.x - lam.y * lam.y, l2i = 2.f * lam.x * lam.y;
            const float l4r = l2r * l2r - l2i * l2i, l4i = 2.f * l2r * l2i;
            const float l8r = l4r * l4r - l4i * l4i, l8i = 2.f * l4r * l4i;
            const float l16r = l8r * l8r - l8i * l8i, l16i = 2.f * l8r * l8i;
            float mr = (fq2 & 1) ? 1.f : l4r, mim = (fq2 & 1) ? 0.f : l4i;
            if (!(fq2 & 2)) { const float a_ = mr * l8r - mim * l8i, b_ = mr * l8i + mim * l8r; mr = a_; mim = b_; }
            if (!hi) { const float a_ = mr * l2r - mim * l2i, b_ = mr * l2i + mim * l2r; mr = a_; mim = b_; }
            const float tr_ = l16r * ger[jj] - l16i * gei[jj] + er, ti_ = l16r * gei[jj] + l16i * ger[jj] + ei;
            float sr = mr * tr_ - mim * ti_, si = mr * ti_ + mim * tr_;
            sr += __shfl_xor(sr, 8); si += __shfl_xor(si, 8);
            sr += __shfl_xor(sr, 16); si += __shfl_xor(si, 16);
            sr += __shfl_xor(sr, 32); si += __shfl_xor(si, 32);
            if (lane2 < 8) Eo[(size_t)(((brow + wr * 64) >> 5) + (mi >> 1)) * PP + pbase + jj * 8 + f8] = make_float2(sr, si);
          }
        }
      }
      asm volatile("s_waitcnt lgkmcnt(0)" ::: "memory");
      const int c0 = nf * 4;
      float* drow = (float*)(bu + (size_t)(brow + wr * 64 + mi * 16) * PP + pbase);
#pragma unroll
      for (int i = 0; i < 9; ++i) {
        const int q = i * 64 + lane2, row = q / 36, ch = q % 36;
        if (ch >= c0) {
          f32x4 v = *(const f32x4*)(wb + row * EB_LD + ch * 16);
          *(f32x4*)(drow + (size_t)row * PP * 2 + ch * 4) = v;
        }
      }
    }
  }
}

__global__ __launch_bounds__(256) void scanA_kernel(char* __restrict__ ws) {
  const int p = (blockIdx.x & 3) * 256 + threadIdx.x, c = blockIdx.x >> 2;
  const float2 lam = ((const float2*)(ws + OFF_LAM))[p];
  const float2* bu = (const float2*)(ws + OFF_BU) + (size_t)c * SCAN_T * PP + p;
  float er = 0.f, ei = 0.f;
#pragma unroll 8
  for (int s = 0; s < SCAN_T; ++s) {
    float2 b = bu[(size_t)s * PP];
    float nr = lam.x * er - lam.y * ei + b.x, ni = lam.x * ei + lam.y * er + b.y;
    er = nr; ei = ni;
  }
  ((float2*)(ws + OFF_E))[(size_t)c * PP + p] = make_float2(er, ei);
}

__global__ __launch_bounds__(256) void scanC_kernel(char* __restrict__ ws) {
  const int p = (blockIdx.x & 3) * 256 + threadIdx.x, c = blockIdx.x >> 2;
  const float2* bu = (const float2*)(ws + OFF_BU) + (size_t)c * SCAN_T * PP + p;
  float2 b[SCAN_T];
#pragma unroll
  for (int s = 0; s < SCAN_T; ++s) b[s] = bu[(size_t)s * PP];
  const float2 lam = ((const float2*)(ws + OFF_LAM))[p];
  const float2* E = (const float2*)(ws + OFF_E) + p;
  float lr = lam.x, li = lam.y;
#pragma unroll
  for (int i = 0; i < 5; ++i) { float a = lr * lr - li * li, bb = 2.f * lr * li; lr = a; li = bb; }
  float hr = 0.f, hi = 0.f;
  {
    float2 e[NCHUNK - 1];
#pragma unroll
    for (int u = 0; u < NCHUNK - 1; ++u) e[u] = (u < c) ? E[(size_t)u * PP] : make_float2(0.f, 0.f);
#pragma unroll
    for (int u = 0; u < NCHUNK - 1; ++u) if (u < c) { float nr = lr * hr - li * hi + e[u].x, ni = lr * hi + li * hr + e[u].y; hr = nr; hi = ni; }
  }
  unsigned short* ah = (unsigned short*)(ws + OFF_AH) + (size_t)c * SCAN_T * KAH + FF + p;
#pragma unroll
  for (int s = 0; s < SCAN_T; ++s) {
    float nr = lam.x * hr - lam.y * hi + b[s].x, ni = lam.x * hi + lam.y * hr + b[s].y;
    hr = nr; hi = ni;
    ah[(size_t)s * KAH] = f2bf(hr);
    ah[(size_t)s * KAH + PP] = f2bf(hi);
  }
}

constexpr int G2_BM = 128, G2_BN = 64, G2_CH = G2_BN / 8;
constexpr int G2_A_BYTES = G2_BM * 64 * 2, G2_B_BYTES = 64 * G2_BN * 2, G2_STAGE = G2_A_BYTES + G2_B_BYTES;
constexpr int G2_NST = 5;
__device__ __forceinline__ int g2_swz(int k) { return (((k >> 1) & 1) << 1) | (((k >> 3) & 1) << 2); }

__global__ __launch_bounds__(256) void gemm2_kernel(
    const bf16* __restrict__ x, const bf16* __restrict__ wd, const bf16* __restrict__ dskip,
    const char* __restrict__ ws, float* __restrict__ out) {
  extern __shared__ __attribute__((aligned(16))) char smem[];
  const int tid = threadIdx.x, lane = tid & 63, wid = __builtin_amdgcn_readfirstlane(tid >> 6);
  const int bid = blockIdx.x;
  const int xcd = bid & 7, idx = bid >> 3;
  const int pm = (xcd & 3) * 4 + (idx & 3), pn = (xcd >> 2) * 8 + (idx >> 2);
  const int brow = pm * G2_BM, bcol = pn * G2_BN;
  const unsigned short* ah = (const unsigned short*)(ws + OFF_AH);
  const unsigned short* ct = (const unsigned short*)(ws + OFF_CT);
  const unsigned short* asrc[4];
#pragma unroll
  for (int i = 0; i < 4; ++i) {
    int R = (i * 4 + wid) * 8 + (lane >> 3), C = ((lane & 7) ^ (R & 7)) * 8;
    asrc[i] = ah + (size_t)(brow + R) * KAH + C;
  }
  int boffs[2];
#pragma unroll
  for (int i = 0; i < 2; ++i) {
    int Lc = (i * 4 + wid) * 64 + lane, k = Lc / G2_CH, cp = Lc % G2_CH, c = cp ^ g2_swz(k);
    boffs[i] = k * DM + bcol + c * 8;
  }
#define ABL_A(x) x
#define ABL_B(x) x
#define G2_STAGE_LOADS(buf, kt) do { \
    char* sa_ = smem + (buf) * G2_STAGE; char* sb_ = sa_ + G2_A_BYTES; \
    ABL_A(_Pragma("unroll") for (int i = 0; i < 4; ++i) GLDS16(asrc[i] + (kt) * 64, sa_ + (i * 4 + wid) * 1024);) \
    const unsigned short* bb_ = (kt) < 56 ? (const unsigned short*)wd + (size_t)(kt) * 64 * DM : ct + (size_t)((kt) - 56) * 64 * DM; \
    ABL_B(_Pragma("unroll") for (int i = 0; i < 2; ++i) GLDS16(bb_ + boffs[i], sb_ + (i * 4 + wid) * 1024);) \
  } while (0)
  const int wr = wid >> 1, wc = wid & 1;
  const int fr = lane & 15, fq = lane >> 4, tq = (lane & 15) >> 2, tp = lane & 3;
  f32x4 accd[4][2], accy[4][2];
#pragma unroll
  for (int i = 0; i < 4; ++i)
#pragma unroll
    for (int j = 0; j < 2; ++j) { accd[i][j] = (f32x4){0.f, 0.f, 0.f, 0.f}; accy[i][j] = (f32x4){0.f, 0.f, 0.f, 0.f}; }

  const unsigned ldsb = (unsigned)(size_t)(__attribute__((address_space(3))) char*)smem;
  const unsigned aA0 = ldsb + (wr * 64 + fr) * 128 + ((fq ^ (fr & 7)) * 16);
  const unsigned aA1 = ldsb + (wr * 64 + fr) * 128 + (((4 + fq) ^ (fr & 7)) * 16);
  unsigned aB[2][2];
#pragma unroll
  for (int nj = 0; nj < 2; ++nj)
#pragma unroll
    for (int t = 0; t < 2; ++t) {
      const int k0 = 8 * fq + tq + 4 * t, j = wc * 2 + nj;
      aB[nj][t] = ldsb + G2_A_BYTES + (k0 * G2_CH + ((2 * j + (tp >> 1)) ^ g2_swz(k0))) * 16 + 8 * (tp & 1);
    }
#define G2_RD(af, bfm, buf, kk) do { \
    bfm[0] = join_frag(lds_tr64<(kk) * 32 * G2_CH * 16>(aB[0][0] + (buf) * G2_STAGE), lds_tr64<(kk) * 32 * G2_CH * 16>(aB[0][1] + (buf) * G2_STAGE)); \
    bfm[1] = join_frag(lds_tr64<(kk) * 32 * G2_CH * 16>(aB[1][0] + (buf) * G2_STAGE), lds_tr64<(kk) * 32 * G2_CH * 16>(aB[1][1] + (buf) * G2_STAGE)); \
    af[0] = lds_b128<0>(((kk) ? aA1 : aA0) + (buf) * G2_STAGE); af[1] = lds_b128<2048>(((kk) ? aA1 : aA0) + (buf) * G2_STAGE); \
    af[2] = lds_b128<4096>(((kk) ? aA1 : aA0) + (buf) * G2_STAGE); af[3] = lds_b128<6144>(((kk) ? aA1 : aA0) + (buf) * G2_STAGE); \
  } while (0)
#define G2_MMA(acc, af, bfm) do { \
    _Pragma("unroll") for (int mi = 0; mi < 4; ++mi) \
      _Pragma("unroll") for (int nj = 0; nj < 2; ++nj) acc[mi][nj] = __builtin_amdgcn_mfma_f32_16x16x32_bf16(af[mi], bfm[nj], acc[mi][nj], 0, 0, 0); \
  } while (0)
#define compute(buf, acc) do { \
    bf16x8 a0[4], a1[4], f0[2], f1[2]; \
    G2_RD(a0, f0, buf, 0); \
    WAIT_LGKM(0); \
    G2_RD(a1, f1, buf, 1); \
    G2_MMA(acc, a0, f0); \
    WAIT_LGKM(0); \
    G2_MMA(acc, a1, f1); \
  } while (0)
  G2_STAGE_LOADS(0, 0); G2_STAGE_LOADS(1, 1); G2_STAGE_LOADS(2, 2); G2_STAGE_LOADS(3, 3);
#define G2_STEP(u, kt, acc) do { WAIT_VM(18); RAW_BAR(); G2_STAGE_LOADS(((u) + 4) % 5, (kt) + 4); compute(u, acc); } while (0)
  for (int kt0 = 0; kt0 < 55; kt0 += 5) {
    G2_STEP(0, kt0, accd); G2_STEP(1, kt0 + 1, accd); G2_STEP(2, kt0 + 2, accd); G2_STEP(3, kt0 + 3, accd); G2_STEP(4, kt0 + 4, accd);
  }
  WAIT_VM(18); RAW_BAR(); G2_STAGE_LOADS(4, 59); compute(0, accd);
  for (int kt0 = 56; kt0 < 81; kt0 += 5) {
    G2_STEP(1, kt0, accy); G2_STEP(2, kt0 + 1, accy); G2_STEP(3, kt0 + 2, accy); G2_STEP(4, kt0 + 3, accy); G2_STEP(0, kt0 + 4, accy);
  }
  WAIT_VM(18); RAW_BAR(); G2_STAGE_LOADS(0, 85); compute(1, accy);
  WAIT_VM(18); RAW_BAR(); G2_STAGE_LOADS(1, 86); compute(2, accy);
  WAIT_VM(18); RAW_BAR(); G2_STAGE_LOADS(2, 87); compute(3, accy);
  WAIT_VM(18); RAW_BAR(); compute(4, accy);
  WAIT_VM(12); RAW_BAR(); compute(0, accy);
  WAIT_VM(6); RAW_BAR(); compute(1, accy);
  WAIT_VM(0); RAW_BAR(); compute(2, accy);
  const unsigned short* xs = (const unsigned short*)x;
  const unsigned short* xn = (const unsigned short*)(ws + OFF_XN);
  WAIT_VM(0); RAW_BAR();
  constexpr int EP_LD = 32 * 8 + 16;
  char* wl = smem + wid * (64 * EP_LD);
#pragma unroll
  for (int nj = 0; nj < 2; ++nj)
#pragma unroll
    for (int mi = 0; mi < 4; ++mi)
#pragma unroll
      for (int r = 0; r < 4; ++r) {
        const float dn = rbf(accd[mi][nj][r]);
        const float g = rbf(dn * __builtin_amdgcn_rcpf(1.0f + __expf(-dn)));
        *(float2*)(wl + (mi * 16 + fq * 4 + r) * EP_LD + (nj * 16 + fr) * 8) = make_float2(2.0f * accy[mi][nj][r], g);
      }
  asm volatile("s_waitcnt lgkmcnt(0)" ::: "memory");
  {
    const int c4 = (lane & 7) * 4, rsub = lane >> 3;
    const int col = bcol + wc * 32 + c4;
    const unsigned short* dk = (const unsigned short*)dskip + col;
    const bf16x4 dk4 = *(const bf16x4*)dk;
#pragma unroll
    for (int i = 0; i < 8; ++i) {
      const int rl = i * 8 + rsub, row = brow + wr * 64 + rl;
      const size_t o = (size_t)row * DM + col;
      const bf16x4 x4 = *(const bf16x4*)(xs + o), n4 = *(const bf16x4*)(xn + (size_t)row * LDXN + col);
      const f32x4 p0 = *(const f32x4*)(wl + rl * EP_LD + c4 * 8), p1 = *(const f32x4*)(wl + rl * EP_LD + c4 * 8 + 16);
      f32x4 res;
      res[0] = bf2f((unsigned short)x4[0]) + (p0[0] + rbf(bf2f((unsigned short)n4[0]) * bf2f((unsigned short)dk4[0]))) * p0[1];
      res[1] = bf2f((unsigned short)x4[1]) + (p0[2] + rbf(bf2f((unsigned short)n4[1]) * bf2f((unsigned short)dk4[1]))) * p0[3];
      res[2] = bf2f((unsigned short)x4[2]) + (p1[0] + rbf(bf2f((unsigned short)n4[2]) * bf2f((unsigned short)dk4[2]))) * p1[1];
      res[3] = bf2f((unsigned short)x4[3]) + (p1[2] + rbf(bf2f((unsigned short)n4[3]) * bf2f((unsigned short)dk4[3]))) * p1[3];
      *(f32x4*)(out + o) = res;
    }
  }
}

extern "C" void kernel_launch(void* const* d_in, const int* in_sizes, int n_in,
                              void* d_out, int out_size, void* d_ws, size_t ws_size,
                              hipStream_t stream) {
  const bf16* x = (const bf16*)d_in[0];
  const bf16* lam_re = (const bf16*)d_in[1];
  const bf16* lam_im = (const bf16*)d_in[2];
  const bf16* b_re = (const bf16*)d_in[3];
  const bf16* b_im = (const bf16*)d_in[4];
  const bf16* c_re = (const bf16*)d_in[5];
  const bf16* c_im = (const bf16*)d_in[6];
  const bf16* log_dt = (const bf16*)d_in[7];
  const bf16* dskip = (const bf16*)d_in[8];
  const bf16* wg = (const bf16*)d_in[10];
  const bf16* wu = (const bf16*)d_in[11];
  const bf16* wd = (const bf16*)d_in[12];
  char* ws = (char*)d_ws;
  float* out = (float*)d_out;

  hipFuncSetAttribute((const void*)gemm1_kernel, hipFuncAttributeMaxDynamicSharedMemorySize, G1_NST * G1_STAGE);
  hipFuncSetAttribute((const void*)gemm2_kernel, hipFuncAttributeMaxDynamicSharedMemorySize, G2_NST * G2_STAGE);

  prep_kernel<<<PREP_BLOCKS, 256, 0, stream>>>(x, lam_re, lam_im, b_re, b_im, c_re, c_im, log_dt, ws);
  gemm1_kernel<<<256, 512, G1_NST * G1_STAGE, stream>>>(wg, wu, ws);
  scanC_kernel<<<4 * NCHUNK, 256, 0, stream>>>(ws);
  gemm2_kernel<<<256, 256, G2_NST * G2_STAGE, stream>>>(x, wd, dskip, ws, out);
}
```

```cpp
#include <hip/hip_runtime.h>
#include <hip/hip_bf16.h>
#include <stdint.h>

typedef __hip_bfloat16 bf16;
typedef __attribute__((ext_vector_type(8))) short bf16x8;
typedef __attribute__((ext_vector_type(4))) short bf16x4;
typedef __attribute__((ext_vector_type(4))) float f32x4;
typedef __attribute__((__vector_size__(4 * sizeof(short)))) short v4s;

constexpr int L_SEQ = 2048, DM = 1024, FF = 3584, PP = 1024;
constexpr int LDW = 3584;
constexpr int LDXN = DM + 64;
constexpr int KAH = FF + 2 * PP + 64;
constexpr int SCAN_T = 32, NCHUNK = L_SEQ / SCAN_T;

constexpr size_t OFF_XN   = 0;
constexpr size_t OFF_BT   = OFF_XN + (size_t)L_SEQ * LDXN * 2;
constexpr size_t OFF_CT   = OFF_BT + (size_t)DM * LDW * 2;
constexpr size_t OFF_AH   = OFF_CT + (size_t)2 * PP * DM * 2;
constexpr size_t OFF_BU   = OFF_AH + (size_t)L_SEQ * KAH * 2;
constexpr size_t OFF_E    = OFF_BU + (size_t)L_SEQ * PP * 8;
constexpr size_t OFF_LAM  = OFF_E + (size_t)NCHUNK * PP * 8;
constexpr size_t OFF_COEF = OFF_LAM + (size_t)PP * 8;

__device__ __forceinline__ float bf2f(unsigned short u) { return __uint_as_float(((unsigned)u) << 16); }
__device__ __forceinline__ unsigned short f2bf(float f) {
  __bf16 b = (__bf16)f;
  return __builtin_bit_cast(unsigned short, b);
}
__device__ __forceinline__ float rbf(float f) { return bf2f(f2bf(f)); }

constexpr int PREP_LN_BLOCKS = L_SEQ / 4;
constexpr int PREP_TR_BLOCKS = 4 * (1024 / 64) * (1024 / 64);
constexpr int PREP_BLOCKS = PREP_LN_BLOCKS + PREP_TR_BLOCKS + 4;

__global__ __launch_bounds__(256) void prep_kernel(
    const bf16* __restrict__ x, const bf16* __restrict__ lam_re, const bf16* __restrict__ lam_im,
    const bf16* __restrict__ b_re, const bf16* __restrict__ b_im,
    const bf16* __restrict__ c_re, const bf16* __restrict__ c_im,
    const bf16* __restrict__ log_dt, char* __restrict__ ws) {
  __shared__ unsigned short tile[64][66];
  const int bid = blockIdx.x, tid = threadIdx.x;
  if (bid < PREP_LN_BLOCKS) {
    const int wave = tid >> 6, lane = tid & 63;
    const int row = bid * 4 + wave;
    const unsigned short* xr = (const unsigned short*)x + (size_t)row * DM;
    float v[16];
    bf16x8 a = *(const bf16x8*)(xr + lane * 8);
    bf16x8 b = *(const bf16x8*)(xr + 512 + lane * 8);
    float sum = 0.f;
#pragma unroll
    for (int j = 0; j < 8; ++j) { v[j] = bf2f((unsigned short)a[j]); v[8 + j] = bf2f((unsigned short)b[j]); }
#pragma unroll
    for (int j = 0; j < 16; ++j) sum += v[j];
#pragma unroll
    for (int o = 32; o > 0; o >>= 1) sum += __shfl_xor(sum, o);
    const float mean = sum * (1.0f / DM);
    float sq = 0.f;
#pragma unroll
    for (int j = 0; j < 16; ++j) { float d = v[j] - mean; sq += d * d; }
#pragma unroll
    for (int o = 32; o > 0; o >>= 1) sq += __shfl_xor(sq, o);
    const float var_b = rbf(sq * (1.0f / DM));
    const float mean_b = rbf(mean);
    const float den = rbf(sqrtf(rbf(var_b + 1e-5f)));
    bf16x8 oa, ob;
#pragma unroll
    for (int j = 0; j < 8; ++j) {
      oa[j] = (short)f2bf(rbf(v[j] - mean_b) / den);
      ob[j] = (short)f2bf(rbf(v[8 + j] - mean_b) / den);
    }
    unsigned short* xo = (unsigned short*)(ws + OFF_XN) + (size_t)row * LDXN;
    *(bf16x8*)(xo + lane * 8) = oa;
    *(bf16x8*)(xo + 512 + lane * 8) = ob;
  } else if (bid < PREP_LN_BLOCKS + PREP_TR_BLOCKS) {
    const int t = bid - PREP_LN_BLOCKS;
    const int mat = t >> 8, ti = (t >> 4) & 15, tj = t & 15;
    const unsigned short* src = (const unsigned short*)(mat == 0 ? b_re : mat == 1 ? b_im : mat == 2 ? c_re : c_im);
    const int tc = tid & 7, tr = tid >> 3;
#pragma unroll
    for (int i = 0; i < 2; ++i) {
      const int r = tr + 32 * i;
      bf16x8 v = *(const bf16x8*)(src + (size_t)(ti * 64 + r) * 1024 + tj * 64 + tc * 8);
#pragma unroll
      for (int e = 0; e < 8; ++e) tile[r][tc * 8 + e] = (unsigned short)v[e];
    }
    __syncthreads();
    unsigned short* dst; size_t ld; unsigned short neg = 0;
    if (mat < 2) { dst = (unsigned short*)(ws + OFF_BT) + mat * 1024; ld = LDW; }
    else { dst = (unsigned short*)(ws + OFF_CT) + (size_t)(mat - 2) * 1024 * DM; ld = DM; neg = (mat == 3) ? 0x8000 : 0; }
#pragma unroll
    for (int i = 0; i < 2; ++i) {
      const int c = tr + 32 * i;
      bf16x8 v;
#pragma unroll
      for (int e = 0; e < 8; ++e) v[e] = (short)(tile[tc * 8 + e][c] ^ neg);
      *(bf16x8*)(dst + (size_t)(tj * 64 + c) * ld + ti * 64 + tc * 8) = v;
    }
  } else {
    const int p = (bid - PREP_LN_BLOCKS - PREP_TR_BLOCKS) * 256 + tid;
    const float lr = bf2f(((const unsigned short*)lam_re)[p]);
    const float li = bf2f(((const unsigned short*)lam_im)[p]);
    const float dt = rbf(expf(bf2f(((const unsigned short*)log_dt)[p])));
    const float ar = dt * lr, ai = dt * li;
    const float e = expf(ar);
    float sn, cs; sincosf(ai, &sn, &cs);
    const float br_ = e * cs, bi_ = e * sn;
    const float nr = br_ - 1.0f, ni = bi_;
    const float den = lr * lr + li * li;
    const float cr = (nr * lr + ni * li) / den, ci = (ni * lr - nr * li) / den;
    ((float2*)(ws + OFF_LAM))[p] = make_float2(br_, bi_);
    ((float2*)(ws + OFF_COEF))[p] = make_float2(cr, ci);
  }
}

__device__ __forceinline__ int a_lds_byte(int r, int c) {
  int st = (r >> 4) * 2 + (c >> 5), rr = r & 15, cc = c & 31, ob = rr * 64 + cc * 2;
  return st * 1024 + (ob ^ (((ob >> 9) & 1) << 5));
}
__device__ __forceinline__ void a_stage_rc(int b, int& R, int& C) {
  int st = b >> 10, sb = b & 1023, swz = sb ^ (((sb >> 9) & 1) << 5);
  R = (st >> 1) * 16 + (swz >> 6); C = (st & 1) * 32 + ((swz & 63) >> 1);
}
__device__ __forceinline__ bf16x8 tr_frag(const char* lds_lo, const char* lds_hi) {
  v4s lo = __builtin_amdgcn_ds_read_tr16_b64_v4i16((__attribute__((address_space(3))) v4s*)(lds_lo));
  v4s hi = __builtin_amdgcn_ds_read_tr16_b64_v4i16((__attribute__((address_space(3))) v4s*)(lds_hi));
  bf16x8 r;
  r[0] = lo[0]; r[1] = lo[1]; r[2] = lo[2]; r[3] = lo[3];
  r[4] = hi[0]; r[5] = hi[1]; r[6] = hi[2]; r[7] = hi[3];
  return r;
}
typedef __attribute__((ext_vector_type(2))) unsigned u32x2;
typedef __attribute__((ext_vector_type(4))) unsigned u32x4;
template <int OFF> __device__ __forceinline__ u32x2 lds_tr64(unsigned addr) {
  u32x2 r; asm volatile("ds_read_b64_tr_b16 %0, %1 offset:%2" : "=v"(r) : "v"(addr), "i"(OFF) : "memory"); return r;
}
template <int OFF> __device__ __forceinline__ bf16x8 lds_b128(unsigned addr) {
  u32x4 r; asm volatile("ds_read_b128 %0, %1 offset:%2" : "=v"(r) : "v"(addr), "i"(OFF) : "memory"); return __builtin_bit_cast(bf16x8, r);
}
__device__ __forceinline__ bf16x8 join_frag(u32x2 lo, u32x2 hi) {
  u32x4 r = __builtin_shufflevector(lo, hi, 0, 1, 2, 3); return __builtin_bit_cast(bf16x8, r);
}
#define WAIT_LGKM(n) do { asm volatile("s_waitcnt lgkmcnt(" #n ")" ::: "memory"); __builtin_amdgcn_sched_barrier(0); } while (0)
#define GLDS16(gptr, ldsptr) __builtin_amdgcn_global_load_lds((const void __attribute__((address_space(1)))*)(gptr), (void __attribute__((address_space(3)))*)(ldsptr), 16, 0, 0)

__device__ __forceinline__ float gelu_tanh_f(float x) {
  const float k0 = 0.7978845608028654f, k1 = 0.044715f;
  float u = k0 * (x + k1 * x * x * x);
  return x * __builtin_amdgcn_rcpf(1.0f + __expf(-2.0f * u));
}

constexpr int G1_BM = 256, G1_BN = 288, G1_CH = G1_BN / 8;
constexpr int G1_BK = 32, G1_NST = 4, G1_NK = 1024 / G1_BK;
constexpr int G1_A_BYTES = G1_BM * G1_BK * 2, G1_B_BYTES = G1_BK * G1_BN * 2, G1_STAGE = G1_A_BYTES + G1_B_BYTES;
__device__ __forceinline__ int g1_swz(int k) { return ((k >> 3) & 1) << 1; }
#define WAIT_VM(n) asm volatile("s_waitcnt vmcnt(" #n ") lgkmcnt(0)" ::: "memory")
#define RAW_BAR() __builtin_amdgcn_s_barrier()

__global__ __launch_bounds__(512, 2) void gemm1_kernel(
    const bf16* __restrict__ wg, const bf16* __restrict__ wu, char* __restrict__ ws) {
  extern __shared__ __attribute__((aligned(16))) char smem[];
  const int tid = threadIdx.x, lane = tid & 63, wid = __builtin_amdgcn_readfirstlane(tid >> 6);
  const int bid = blockIdx.x;
  const int xcd = bid & 7, idx = bid >> 3;
  const int pn = xcd * 4 + (idx & 3), pm = idx >> 2;
  const int brow = pm * G1_BM;
  const unsigned short* xn = (const unsigned short*)(ws + OFF_XN);
  const unsigned short* bt = (const unsigned short*)(ws + OFF_BT);

  unsigned aoffs[2];
#pragma unroll
  for (int i = 0; i < 2; ++i) {
    int sb = lane * 16, swz = sb ^ (((sb >> 9) & 1) << 5);
    int R = (i * 8 + wid) * 16 + (swz >> 6), C = (swz & 63) >> 1;
    aoffs[i] = (unsigned)((brow + R) * LDXN + C);
  }
  const unsigned short* bsrc[3];
#pragma unroll
  for (int i = 0; i < 3; ++i) {
    int inst = i * 8 + wid; if (inst > 17) inst = 17;
    int Lc = inst * 64 + lane, k = Lc / G1_CH, cp = Lc % G1_CH, c = cp ^ g1_swz(k);
    int half = c >= 18, cc = c - half * 18, grp = pn * 9 + (cc >> 1), col = (cc & 1) * 8;
    const unsigned short* base = (const unsigned short*)(half ? wu : wg);
    int o = grp * 16;
    if (grp >= 224) { base = bt; o = half * 1024 + (grp - 224) * 16; }
    bsrc[i] = base + (k * LDW + col + o);
  }
#define G1_STAGE_LOADS(buf) do { \
    char* sa_ = smem + (buf) * G1_STAGE; char* sb_ = sa_ + G1_A_BYTES; \
    _Pragma("unroll") for (int i = 0; i < 2; ++i) { GLDS16(xn + aoffs[i], sa_ + (i * 8 + wid) * 1024); aoffs[i] += G1_BK; } \
    _Pragma("unroll") for (int i = 0; i < 2; ++i) { GLDS16(bsrc[i], sb_ + (i * 8 + wid) * 1024); bsrc[i] += G1_BK * LDW; } \
    if (wid < 2) { GLDS16(bsrc[2], sb_ + (16 + wid) * 1024); bsrc[2] += G1_BK * LDW; } \
  } while (0)
  f32x4 acc[2][18];
#pragma unroll
  for (int i = 0; i < 2; ++i)
#pragma unroll
    for (int j = 0; j < 18; ++j) acc[i][j] = (f32x4){0.f, 0.f, 0.f, 0.f};

  const int fr = lane & 15, fq = lane >> 4, tq = (lane & 15) >> 2, tp = lane & 3;
  const int ob_ = fr * 64 + fq * 16, aoff0 = (wid * 2) * 1024 + (ob_ ^ (((ob_ >> 9) & 1) << 5));
  const int bRow = G1_A_BYTES + ((8 * fq + tq) * G1_CH + (tp >> 1)) * 16 + 8 * (tp & 1);
  const int bEven = bRow + 32 * (fq & 1), bOdd = bRow - 32 * (fq & 1);

  const unsigned ldsb = (unsigned)(size_t)(__attribute__((address_space(3))) char*)smem;
  const unsigned aA = ldsb + aoff0, aE = ldsb + bEven, aO = ldsb + bOdd;
#define G1_RDB(dst, buf, jg) do { \
    dst[0] = join_frag(lds_tr64<32 * ((jg) * 6 + 0)>(aE + (buf) * G1_STAGE), lds_tr64<32 * ((jg) * 6 + 0) + 4 * G1_CH * 16>(aE + (buf) * G1_STAGE)); \
    dst[1] = join_frag(lds_tr64<32 * ((jg) * 6 + 1)>(aO + (buf) * G1_STAGE), lds_tr64<32 * ((jg) * 6 + 1) + 4 * G1_CH * 16>(aO + (buf) * G1_STAGE)); \
    dst[2] = join_frag(lds_tr64<32 * ((jg) * 6 + 2)>(aE + (buf) * G1_STAGE), lds_tr64<32 * ((jg) * 6 + 2) + 4 * G1_CH * 16>(aE + (buf) * G1_STAGE)); \
    dst[3] = join_frag(lds_tr64<32 * ((jg) * 6 + 3)>(aO + (buf) * G1_STAGE), lds_tr64<32 * ((jg) * 6 + 3) + 4 * G1_CH * 16>(aO + (buf) * G1_STAGE)); \
    dst[4] = join_frag(lds_tr64<32 * ((jg) * 6 + 4)>(aE + (buf) * G1_STAGE), lds_tr64<32 * ((jg) * 6 + 4) + 4 * G1_CH * 16>(aE + (buf) * G1_STAGE)); \
    dst[5] = join_frag(lds_tr64<32 * ((jg) * 6 + 5)>(aO + (buf) * G1_STAGE), lds_tr64<32 * ((jg) * 6 + 5) + 4 * G1_CH * 16>(aO + (buf) * G1_STAGE)); \
  } while (0)
#define G1_MMA(src, jg) do { \
    _Pragma("unroll") for (int jj = 0; jj < 6; ++jj) \
      _Pragma("unroll") for (int mi = 0; mi < 2; ++mi) \
        acc[mi][(jg) * 6 + jj] = __builtin_amdgcn_mfma_f32_16x16x32_bf16(af[mi], src[jj], acc[mi][(jg) * 6 + jj], 0, 0, 0); \
  } while (0)
#define G1_COMPUTE(buf) do { \
    bf16x8 af[2], b0[6], b1[6]; \
    af[0] = lds_b128<0>(aA + (buf) * G1_STAGE); af[1] = lds_b128<1024>(aA + (buf) * G1_STAGE); \
    G1_RDB(b0, buf, 0); \
    WAIT_LGKM(0); \
    G1_RDB(b1, buf, 1); \
    G1_MMA(b0, 0); \
    WAIT_LGKM(0); \
    G1_RDB(b0, buf, 2); \
    G1_MMA(b1, 1); \
    WAIT_LGKM(0); \
    G1_MMA(b0, 2); \
  } while (0)

  G1_STAGE_LOADS(0); G1_STAGE_LOADS(1); G1_STAGE_LOADS(2);
#define G1_STEP(u) do { if (wid < 2) WAIT_VM(10); else WAIT_VM(8); RAW_BAR(); G1_STAGE_LOADS(((u) + 3) & 3); G1_COMPUTE(u); } while (0)
  for (int kt0 = 0; kt0 < G1_NK - 4; kt0 += 4) {
    G1_STEP(0); G1_STEP(1); G1_STEP(2); G1_STEP(3);
  }
  if (wid < 2) WAIT_VM(10); else WAIT_VM(8);
  RAW_BAR(); G1_STAGE_LOADS(3); G1_COMPUTE(0);
  if (wid < 2) WAIT_VM(10); else WAIT_VM(8);
  RAW_BAR(); G1_COMPUTE(1);
  if (wid < 2) WAIT_VM(5); else WAIT_VM(4);
  RAW_BAR(); G1_COMPUTE(2);
  WAIT_VM(0);
  RAW_BAR(); G1_COMPUTE(3);
  int tz = threadIdx.x; asm volatile("" : "+v"(tz));
  const int lane2 = tz & 63, fr2 = tz & 15, fq2 = (tz >> 4) & 3;
  unsigned short* ah = (unsigned short*)(ws + OFF_AH);
  float2* bu = (float2*)(ws + OFF_BU);
  const float2* coef = (const float2*)(ws + OFF_COEF);
  const float2* lamb = (const float2*)(ws + OFF_LAM);
  float2* Eo = (float2*)(ws + OFF_E);
  const int nf = min(9, max(0, 224 - pn * 9));
  WAIT_VM(0); RAW_BAR();
  constexpr int EP_LD = 304;
  char* wl = smem + wid * (32 * EP_LD);
#pragma unroll
  for (int j = 0; j < 9; ++j) {
    if (j < nf) {
#pragma unroll
      for (int mi = 0; mi < 2; ++mi)
#pragma unroll
        for (int r = 0; r < 4; ++r) {
          float g = rbf(acc[mi][j][r]), u = rbf(acc[mi][9 + j][r]);
          float a = rbf(gelu_tanh_f(g)) * u;
          *(unsigned short*)(wl + (mi * 16 + fq2 * 4 + r) * EP_LD + (j * 16 + fr2) * 2) = f2bf(a);
        }
    } else {
      const int p = (pn * 9 + j - 224) * 16 + fr2;
      const float2 cf = coef[p];
      const float2 lam = lamb[p];
      float er[2], ei[2];
#pragma unroll
      for (int mi = 0; mi < 2; ++mi) {
        er[mi] = 0.f; ei[mi] = 0.f;
#pragma unroll
        for (int r = 0; r < 4; ++r) {
          const int row = brow + wid * 32 + mi * 16 + fq2 * 4 + r;
          float ur = acc[mi][j][r], ui = acc[mi][9 + j][r];
          const float br = cf.x * ur - cf.y * ui, bi = cf.x * ui + cf.y * ur;
          bu[(size_t)row * PP + p] = make_float2(br, bi);
          const float nr = lam.x * er[mi] - lam.y * ei[mi] + br, ni = lam.x * ei[mi] + lam.y * er[mi] + bi;
          er[mi] = nr; ei[mi] = ni;
        }
      }
      const float l2r = lam.x * lam.x - lam.y * lam.y, l2i = 2.f * lam.x * lam.y;
      const float l4r = l2r * l2r - l2i * l2i, l4i = 2.f * l2r * l2i;
      const float l8r = l4r * l4r - l4i * l4i, l8i = 2.f * l4r * l4i;
      const float l16r = l8r * l8r - l8i * l8i, l16i = 2.f * l8r * l8i;
      float tr = l16r * er[0] - l16i * ei[0] + er[1], ti = l16r * ei[0] + l16i * er[0] + ei[1];
      float mr = (fq2 & 1) ? 1.f : l4r, mi_ = (fq2 & 1) ? 0.f : l4i;
      if (!(fq2 & 2)) { const float a = mr * l8r - mi_ * l8i, b = mr * l8i + mi_ * l8r; mr = a; mi_ = b; }
      float sr = mr * tr - mi_ * ti, si = mr * ti + mi_ * tr;
      sr += __shfl_xor(sr, 16); si += __shfl_xor(si, 16);
      sr += __shfl_xor(sr, 32); si += __shfl_xor(si, 32);
      if (fq2 == 0) Eo[(size_t)((brow >> 5) + wid) * PP + p] = make_float2(sr, si);
    }
  }
  asm volatile("s_waitcnt lgkmcnt(0)" ::: "memory");
  {
    const int nch = nf * 2;
    unsigned short* dst0 = ah + (size_t)(brow + wid * 32) * KAH + pn * 144;
#pragma unroll
    for (int i = 0; i < 9; ++i) {
      const int q = i * 64 + lane2, row = q / 18, ch = q % 18;
      if (ch < nch) {
        bf16x8 v = *(const bf16x8*)(wl + row * EP_LD + ch * 16);
        *(bf16x8*)(dst0 + (size_t)row * KAH + ch * 8) = v;
      }
    }
  }
}

__global__ __launch_bounds__(256) void scanA_kernel(char* __restrict__ ws) {
  const int p = (blockIdx.x & 3) * 256 + threadIdx.x, c = blockIdx.x >> 2;
  const float2 lam = ((const float2*)(ws + OFF_LAM))[p];
  const float2* bu = (const float2*)(ws + OFF_BU) + (size_t)c * SCAN_T * PP + p;
  float er = 0.f, ei = 0.f;
#pragma unroll 8
  for (int s = 0; s < SCAN_T; ++s) {
    float2 b = bu[(size_t)s * PP];
    float nr = lam.x * er - lam.y * ei + b.x, ni = lam.x * ei + lam.y * er + b.y;
    er = nr; ei = ni;
  }
  ((float2*)(ws + OFF_E))[(size_t)c * PP + p] = make_float2(er, ei);
}

__global__ __launch_bounds__(256) void scanC_kernel(char* __restrict__ ws) {
  const int p = (blockIdx.x & 3) * 256 + threadIdx.x, c = blockIdx.x >> 2;
  const float2* bu = (const float2*)(ws + OFF_BU) + (size_t)c * SCAN_T * PP + p;
  float2 b[SCAN_T];
#pragma unroll
  for (int s = 0; s < SCAN_T; ++s) b[s] = bu[(size_t)s * PP];
  const float2 lam = ((const float2*)(ws + OFF_LAM))[p];
  const float2* E = (const float2*)(ws + OFF_E) + p;
  float lr = lam.x, li = lam.y;
#pragma unroll
  for (int i = 0; i < 5; ++i) { float a = lr * lr - li * li, bb = 2.f * lr * li; lr = a; li = bb; }
  float hr = 0.f, hi = 0.f;
  {
    float2 e[NCHUNK - 1];
#pragma unroll
    for (int u = 0; u < NCHUNK - 1; ++u) e[u] = (u < c) ? E[(size_t)u * PP] : make_float2(0.f, 0.f);
#pragma unroll
    for (int u = 0; u < NCHUNK - 1; ++u) if (u < c) { float nr = lr * hr - li * hi + e[u].x, ni = lr * hi + li * hr + e[u].y; hr = nr; hi = ni; }
  }
  unsigned short* ah = (unsigned short*)(ws + OFF_AH) + (size_t)c * SCAN_T * KAH + FF + p;
#pragma unroll
  for (int s = 0; s < SCAN_T; ++s) {
    float nr = lam.x * hr - lam.y * hi + b[s].x, ni = lam.x * hi + lam.y * hr + b[s].y;
    hr = nr; hi = ni;
    ah[(size_t)s * KAH] = f2bf(hr);
    ah[(size_t)s * KAH + PP] = f2bf(hi);
  }
}

constexpr int G2_BM = 128, G2_BN = 64, G2_CH = G2_BN / 8;
constexpr int G2_A_BYTES = G2_BM * 64 * 2, G2_B_BYTES = 64 * G2_BN * 2, G2_STAGE = G2_A_BYTES + G2_B_BYTES;
constexpr int G2_NST = 5;
__device__ __forceinline__ int g2_swz(int k) { return (((k >> 1) & 1) << 1) | (((k >> 3) & 1) << 2); }

__global__ __launch_bounds__(256) void gemm2_kernel(
    const bf16* __restrict__ x, const bf16* __restrict__ wd, const bf16* __restrict__ dskip,
    const char* __restrict__ ws, float* __restrict__ out) {
  extern __shared__ __attribute__((aligned(16))) char smem[];
  const int tid = threadIdx.x, lane = tid & 63, wid = __builtin_amdgcn_readfirstlane(tid >> 6);
  const int bid = blockIdx.x;
  const int xcd = bid & 7, idx = bid >> 3;
  const int pm = (xcd & 3) * 4 + (idx & 3), pn = (xcd >> 2) * 8 + (idx >> 2);
  const int brow = pm * G2_BM, bcol = pn * G2_BN;
  const unsigned short* ah = (const unsigned short*)(ws + OFF_AH);
  const unsigned short* ct = (const unsigned short*)(ws + OFF_CT);
  const unsigned short* asrc[4];
#pragma unroll
  for (int i = 0; i < 4; ++i) {
    int R = (i * 4 + wid) * 8 + (lane >> 3), C = ((lane & 7) ^ (R & 7)) * 8;
    asrc[i] = ah + (size_t)(brow + R) * KAH + C;
  }
  int boffs[2];
#pragma unroll
  for (int i = 0; i < 2; ++i) {
    int Lc = (i * 4 + wid) * 64 + lane, k = Lc / G2_CH, cp = Lc % G2_CH, c = cp ^ g2_swz(k);
    boffs[i] = k * DM + bcol + c * 8;
  }
#define ABL_A(x) x
#define ABL_B(x) x
#define G2_STAGE_LOADS(buf, kt) do { \
    char* sa_ = smem + (buf) * G2_STAGE; char* sb_ = sa_ + G2_A_BYTES; \
    ABL_A(_Pragma("unroll") for (int i = 0; i < 4; ++i) GLDS16(asrc[i] + (kt) * 64, sa_ + (i * 4 + wid) * 1024);) \
    const unsigned short* bb_ = (kt) < 56 ? (const unsigned short*)wd + (size_t)(kt) * 64 * DM : ct + (size_t)((kt) - 56) * 64 * DM; \
    ABL_B(_Pragma("unroll") for (int i = 0; i < 2; ++i) GLDS16(bb_ + boffs[i], sb_ + (i * 4 + wid) * 1024);) \
  } while (0)
  const int wr = wid >> 1, wc = wid & 1;
  const int fr = lane & 15, fq = lane >> 4, tq = (lane & 15) >> 2, tp = lane & 3;
  f32x4 accd[4][2], accy[4][2];
#pragma unroll
  for (int i = 0; i < 4; ++i)
#pragma unroll
    for (int j = 0; j < 2; ++j) { accd[i][j] = (f32x4){0.f, 0.f, 0.f, 0.f}; accy[i][j] = (f32x4){0.f, 0.f, 0.f, 0.f}; }

  const unsigned ldsb = (unsigned)(size_t)(__attribute__((address_space(3))) char*)smem;
  const unsigned aA0 = ldsb + (wr * 64 + fr) * 128 + ((fq ^ (fr & 7)) * 16);
  const unsigned aA1 = ldsb + (wr * 64 + fr) * 128 + (((4 + fq) ^ (fr & 7)) * 16);
  unsigned aB[2][2];
#pragma unroll
  for (int nj = 0; nj < 2; ++nj)
#pragma unroll
    for (int t = 0; t < 2; ++t) {
      const int k0 = 8 * fq + tq + 4 * t, j = wc * 2 + nj;
      aB[nj][t] = ldsb + G2_A_BYTES + (k0 * G2_CH + ((2 * j + (tp >> 1)) ^ g2_swz(k0))) * 16 + 8 * (tp & 1);
    }
#define G2_RD(af, bfm, buf, kk) do { \
    bfm[0] = join_frag(lds_tr64<(kk) * 32 * G2_CH * 16>(aB[0][0] + (buf) * G2_STAGE), lds_tr64<(kk) * 32 * G2_CH * 16>(aB[0][1] + (buf) * G2_STAGE)); \
    bfm[1] = join_frag(lds_tr64<(kk) * 32 * G2_CH * 16>(aB[1][0] + (buf) * G2_STAGE), lds_tr64<(kk) * 32 * G2_CH * 16>(aB[1][1] + (buf) * G2_STAGE)); \
    af[0] = lds_b128<0>(((kk) ? aA1 : aA0) + (buf) * G2_STAGE); af[1] = lds_b128<2048>(((kk) ? aA1 : aA0) + (buf) * G2_STAGE); \
    af[2] = lds_b128<4096>(((kk) ? aA1 : aA0) + (buf) * G2_STAGE); af[3] = lds_b128<6144>(((kk) ? aA1 : aA0) + (buf) * G2_STAGE); \
  } while (0)
#define G2_MMA(acc, af, bfm) do { \
    _Pragma("unroll") for (int mi = 0; mi < 4; ++mi) \
      _Pragma("unroll") for (int nj = 0; nj < 2; ++nj) acc[mi][nj] = __builtin_amdgcn_mfma_f32_16x16x32_bf16(af[mi], bfm[nj], acc[mi][nj], 0, 0, 0); \
  } while (0)
#define compute(buf, acc) do { \
    bf16x8 a0[4], a1[4], f0[2], f1[2]; \
    G2_RD(a0, f0, buf, 0); \
    WAIT_LGKM(0); \
    G2_RD(a1, f1, buf, 1); \
    G2_MMA(acc, a0, f0); \
    WAIT_LGKM(0); \
    G2_MMA(acc, a1, f1); \
  } while (0)
  G2_STAGE_LOADS(0, 0); G2_STAGE_LOADS(1, 1); G2_STAGE_LOADS(2, 2); G2_STAGE_LOADS(3, 3);
#define G2_STEP(u, kt, acc) do { WAIT_VM(18); RAW_BAR(); G2_STAGE_LOADS(((u) + 4) % 5, (kt) + 4); compute(u, acc); } while (0)
  for (int kt0 = 0; kt0 < 55; kt0 += 5) {
    G2_STEP(0, kt0, accd); G2_STEP(1, kt0 + 1, accd); G2_STEP(2, kt0 + 2, accd); G2_STEP(3, kt0 + 3, accd); G2_STEP(4, kt0 + 4, accd);
  }
  WAIT_VM(18); RAW_BAR(); G2_STAGE_LOADS(4, 59); compute(0, accd);
  for (int kt0 = 56; kt0 < 81; kt0 += 5) {
    G2_STEP(1, kt0, accy); G2_STEP(2, kt0 + 1, accy); G2_STEP(3, kt0 + 2, accy); G2_STEP(4, kt0 + 3, accy); G2_STEP(0, kt0 + 4, accy);
  }
  WAIT_VM(18); RAW_BAR(); G2_STAGE_LOADS(0, 85); compute(1, accy);
  WAIT_VM(18); RAW_BAR(); G2_STAGE_LOADS(1, 86); compute(2, accy);
  WAIT_VM(18); RAW_BAR(); G2_STAGE_LOADS(2, 87); compute(3, accy);
  WAIT_VM(18); RAW_BAR(); compute(4, accy);
  WAIT_VM(12); RAW_BAR(); compute(0, accy);
  WAIT_VM(6); RAW_BAR(); compute(1, accy);
  WAIT_VM(0); RAW_BAR(); compute(2, accy);
  const unsigned short* xs = (const unsigned short*)x;
  const unsigned short* xn = (const unsigned short*)(ws + OFF_XN);
  WAIT_VM(0); RAW_BAR();
  constexpr int EP_LD = 32 * 8 + 16;
  char* wl = smem + wid * (64 * EP_LD);
#pragma unroll
  for (int nj = 0; nj < 2; ++nj)
#pragma unroll
    for (int mi = 0; mi < 4; ++mi)
#pragma unroll
      for (int r = 0; r < 4; ++r) {
        const float dn = rbf(accd[mi][nj][r]);
        const float g = rbf(dn * __builtin_amdgcn_rcpf(1.0f + __expf(-dn)));
        *(float2*)(wl + (mi * 16 + fq * 4 + r) * EP_LD + (nj * 16 + fr) * 8) = make_float2(2.0f * accy[mi][nj][r], g);
      }
  asm volatile("s_waitcnt lgkmcnt(0)" ::: "memory");
  {
    const int c4 = (lane & 7) * 4, rsub = lane >> 3;
    const int col = bcol + wc * 32 + c4;
    const unsigned short* dk = (const unsigned short*)dskip + col;
    const bf16x4 dk4 = *(const bf16x4*)dk;
#pragma unroll
    for (int i = 0; i < 8; ++i) {
      const int rl = i * 8 + rsub, row = brow + wr * 64 + rl;
      const size_t o = (size_t)row * DM + col;
      const bf16x4 x4 = *(const bf16x4*)(xs + o), n4 = *(const bf16x4*)(xn + (size_t)row * LDXN + col);
      const f32x4 p0 = *(const f32x4*)(wl + rl * EP_LD + c4 * 8), p1 = *(const f32x4*)(wl + rl * EP_LD + c4 * 8 + 16);
      f32x4 res;
      res[0] = bf2f((unsigned short)x4[0]) + (p0[0] + rbf(bf2f((unsigned short)n4[0]) * bf2f((unsigned short)dk4[0]))) * p0[1];
      res[1] = bf2f((unsigned short)x4[1]) + (p0[2] + rbf(bf2f((unsigned short)n4[1]) * bf2f((unsigned short)dk4[1]))) * p0[3];
      res[2] = bf2f((unsigned short)x4[2]) + (p1[0] + rbf(bf2f((unsigned short)n4[2]) * bf2f((unsigned short)dk4[2]))) * p1[1];
      res[3] = bf2f((unsigned short)x4[3]) + (p1[2] + rbf(bf2f((unsigned short)n4[3]) * bf2f((unsigned short)dk4[3]))) * p1[3];
      *(f32x4*)(out + o) = res;
    }
  }
}

extern "C" void kernel_launch(void* const* d_in, const int* in_sizes, int n_in,
                              void* d_out, int out_size, void* d_ws, size_t ws_size,
                              hipStream_t stream) {
  const bf16* x = (const bf16*)d_in[0];
  const bf16* lam_re = (const bf16*)d_in[1];
  const bf16* lam_im = (const bf16*)d_in[2];
  const bf16* b_re = (const bf16*)d_in[3];
  const bf16* b_im = (const bf16*)d_in[4];
  const bf16* c_re = (const bf16*)d_in[5];
  const bf16* c_im = (const bf16*)d_in[6];
  const bf16* log_dt = (const bf16*)d_in[7];
  const bf16* dskip = (const bf16*)d_in[8];
  const bf16* wg = (const bf16*)d_in[10];
  const bf16* wu = (const bf16*)d_in[11];
  const bf16* wd = (const bf16*)d_in[12];
  char* ws = (char*)d_ws;
  float* out = (float*)d_out;

  hipFuncSetAttribute((const void*)gemm1_kernel, hipFuncAttributeMaxDynamicSharedMemorySize, G1_NST * G1_STAGE);
  hipFuncSetAttribute((const void*)gemm2_kernel, hipFuncAttributeMaxDynamicSharedMemorySize, G2_NST * G2_STAGE);

  prep_kernel<<<PREP_BLOCKS, 256, 0, stream>>>(x, lam_re, lam_im, b_re, b_im, c_re, c_im, log_dt, ws);
  gemm1_kernel<<<256, 512, G1_NST * G1_STAGE, stream>>>(wg, wu, ws);
  scanC_kernel<<<4 * NCHUNK, 256, 0, stream>>>(ws);
  gemm2_kernel<<<256, 256, G2_NST * G2_STAGE, stream>>>(x, wd, dskip, ws, out);
}
```

```cpp
#include <hip/hip_runtime.h>
#include <hip/hip_bf16.h>
#include <stdint.h>

typedef __hip_bfloat16 bf16;
typedef __attribute__((ext_vector_type(8))) short bf16x8;
typedef __attribute__((ext_vector_type(4))) short bf16x4;
typedef __attribute__((ext_vector_type(4))) float f32x4;
typedef __attribute__((__vector_size__(4 * sizeof(short)))) short v4s;

constexpr int L_SEQ = 2048, DM = 1024, FF = 3584, PP = 1024;
constexpr int LDW = 3584;
constexpr int LDXN = DM + 64;
constexpr int KAH = FF + 2 * PP + 64;
constexpr int SCAN_T = 32, NCHUNK = L_SEQ / SCAN_T;

constexpr size_t OFF_XN   = 0;
constexpr size_t OFF_BT   = OFF_XN + (size_t)L_SEQ * LDXN * 2;
constexpr size_t OFF_CT   = OFF_BT + (size_t)DM * LDW * 2;
constexpr size_t OFF_AH   = OFF_CT + (size_t)2 * PP * DM * 2;
constexpr size_t OFF_BU   = OFF_AH + (size_t)L_SEQ * KAH * 2;
constexpr size_t OFF_E    = OFF_BU + (size_t)L_SEQ * PP * 8;
constexpr size_t OFF_LAM  = OFF_E + (size_t)NCHUNK * PP * 8;
constexpr size_t OFF_COEF = OFF_LAM + (size_t)PP * 8;

__device__ __forceinline__ float bf2f(unsigned short u) { return __uint_as_float(((unsigned)u) << 16); }
__device__ __forceinline__ unsigned short f2bf(float f) {
  __bf16 b = (__bf16)f;
  return __builtin_bit_cast(unsigned short, b);
}
__device__ __forceinline__ float rbf(float f) { return bf2f(f2bf(f)); }

constexpr int PREP_LN_BLOCKS = L_SEQ / 4;
constexpr int PREP_TR_BLOCKS = 4 * (1024 / 64) * (1024 / 64);
constexpr int PREP_PF_BLOCKS = 448;
constexpr int PREP_BLOCKS = PREP_LN_BLOCKS + PREP_TR_BLOCKS + 4 + PREP_PF_BLOCKS;

__global__ __launch_bounds__(256) void prep_kernel(
    const bf16* __restrict__ x, const bf16* __restrict__ lam_re, const bf16* __restrict__ lam_im,
    const bf16* __restrict__ b_re, const bf16* __restrict__ b_im,
    const bf16* __restrict__ c_re, const bf16* __restrict__ c_im,
    const bf16* __restrict__ log_dt, char* __restrict__ ws,
    const bf16* __restrict__ wg, const bf16* __restrict__ wu) {
  __shared__ unsigned short tile[64][66];
  const int tid = threadIdx.x;
  if (blockIdx.x < PREP_PF_BLOCKS) {
    const int m = blockIdx.x / 224, part = blockIdx.x % 224;
    const bf16x8* src = (const bf16x8*)(m == 0 ? wg : wu) + (size_t)part * 2048 + tid;
    bf16x8 v[8];
#pragma unroll
    for (int i = 0; i < 8; ++i) v[i] = src[i * 256];
    asm volatile("" :: "v"(v[0]), "v"(v[1]), "v"(v[2]), "v"(v[3]), "v"(v[4]), "v"(v[5]), "v"(v[6]), "v"(v[7]));
    return;
  }
  const int bid = blockIdx.x - PREP_PF_BLOCKS;
  if (bid < PREP_LN_BLOCKS) {
    const int wave = tid >> 6, lane = tid & 63;
    const int row = bid * 4 + wave;
    const unsigned short* xr = (const unsigned short*)x + (size_t)row * DM;
    float v[16];
    bf16x8 a = *(const bf16x8*)(xr + lane * 8);
    bf16x8 b = *(const bf16x8*)(xr + 512 + lane * 8);
    float sum = 0.f;
#pragma unroll
    for (int j = 0; j < 8; ++j) { v[j] = bf2f((unsigned short)a[j]); v[8 + j] = bf2f((unsigned short)b[j]); }
#pragma unroll
    for (int j = 0; j < 16; ++j) sum += v[j];
#pragma unroll
    for (int o = 32; o > 0; o >>= 1) sum += __shfl_xor(sum, o);
    const float mean = sum * (1.0f / DM);
    float sq = 0.f;
#pragma unroll
    for (int j = 0; j < 16; ++j) { float d = v[j] - mean; sq += d * d; }
#pragma unroll
    for (int o = 32; o > 0; o >>= 1) sq += __shfl_xor(sq, o);
    const float var_b = rbf(sq * (1.0f / DM));
    const float mean_b = rbf(mean);
    const float den = rbf(sqrtf(rbf(var_b + 1e-5f)));
    bf16x8 oa, ob;
#pragma unroll
    for (int j = 0; j < 8; ++j) {
      oa[j] = (short)f2bf(rbf(v[j] - mean_b) / den);
      ob[j] = (short)f2bf(rbf(v[8 + j] - mean_b) / den);
    }
    unsigned short* xo = (unsigned short*)(ws + OFF_XN) + (size_t)row * LDXN;
    *(bf16x8*)(xo + lane * 8) = oa;
    *(bf16x8*)(xo + 512 + lane * 8) = ob;
  } else if (bid < PREP_LN_BLOCKS + PREP_TR_BLOCKS) {
    const int t = bid - PREP_LN_BLOCKS;
    const int mat = t >> 8, ti = (t >> 4) & 15, tj = t & 15;
    const unsigned short* src = (const unsigned short*)(mat == 0 ? b_re : mat == 1 ? b_im : mat == 2 ? c_re : c_im);
    const int tc = tid & 7, tr = tid >> 3;
#pragma unroll
    for (int i = 0; i < 2; ++i) {
      const int r = tr + 32 * i;
      bf16x8 v = *(const bf16x8*)(src + (size_t)(ti * 64 + r) * 1024 + tj * 64 + tc * 8);
#pragma unroll
      for (int e = 0; e < 8; ++e) tile[r][tc * 8 + e] = (unsigned short)v[e];
    }
    __syncthreads();
    unsigned short* dst; size_t ld; unsigned short neg = 0;
    if (mat < 2) { dst = (unsigned short*)(ws + OFF_BT) + mat * 1024; ld = LDW; }
    else { dst = (unsigned short*)(ws + OFF_CT) + (size_t)(mat - 2) * 1024 * DM; ld = DM; neg = (mat == 3) ? 0x8000 : 0; }
#pragma unroll
    for (int i = 0; i < 2; ++i) {
      const int c = tr + 32 * i;
      bf16x8 v;
#pragma unroll
      for (int e = 0; e < 8; ++e) v[e] = (short)(tile[tc * 8 + e][c] ^ neg);
      *(bf16x8*)(dst + (size_t)(tj * 64 + c) * ld + ti * 64 + tc * 8) = v;
    }
  } else {
    const int p = (bid - PREP_LN_BLOCKS - PREP_TR_BLOCKS) * 256 + tid;
    const float lr = bf2f(((const unsigned short*)lam_re)[p]);
    const float li = bf2f(((const unsigned short*)lam_im)[p]);
    const float dt = rbf(expf(bf2f(((const unsigned short*)log_dt)[p])));
    const float ar = dt * lr, ai = dt * li;
    const float e = expf(ar);
    float sn, cs; sincosf(ai, &sn, &cs);
    const float br_ = e * cs, bi_ = e * sn;
    const float nr = br_ - 1.0f, ni = bi_;
    const float den = lr * lr + li * li;
    const float cr = (nr * lr + ni * li) / den, ci = (ni * lr - nr * li) / den;
    ((float2*)(ws + OFF_LAM))[p] = make_float2(br_, bi_);
    ((float2*)(ws + OFF_COEF))[p] = make_float2(cr, ci);
  }
}

__device__ __forceinline__ int a_lds_byte(int r, int c) {
  int st = (r >> 4) * 2 + (c >> 5), rr = r & 15, cc = c & 31, ob = rr * 64 + cc * 2;
  return st * 1024 + (ob ^ (((ob >> 9) & 1) << 5));
}
__device__ __forceinline__ void a_stage_rc(int b, int& R, int& C) {
  int st = b >> 10, sb = b & 1023, swz = sb ^ (((sb >> 9) & 1) << 5);
  R = (st >> 1) * 16 + (swz >> 6); C = (st & 1) * 32 + ((swz & 63) >> 1);
}
__device__ __forceinline__ bf16x8 tr_frag(const char* lds_lo, const char* lds_hi) {
  v4s lo = __builtin_amdgcn_ds_read_tr16_b64_v4i16((__attribute__((address_space(3))) v4s*)(lds_lo));
  v4s hi = __builtin_amdgcn_ds_read_tr16_b64_v4i16((__attribute__((address_space(3))) v4s*)(lds_hi));
  bf16x8 r;
  r[0] = lo[0]; r[1] = lo[1]; r[2] = lo[2]; r[3] = lo[3];
  r[4] = hi[0]; r[5] = hi[1]; r[6] = hi[2]; r[7] = hi[3];
  return r;
}
typedef __attribute__((ext_vector_type(2))) unsigned u32x2;
typedef __attribute__((ext_vector_type(4))) unsigned u32x4;
template <int OFF> __device__ __forceinline__ u32x2 lds_tr64(unsigned addr) {
  u32x2 r; asm volatile("ds_read_b64_tr_b16 %0, %1 offset:%2" : "=v"(r) : "v"(addr), "i"(OFF) : "memory"); return r;
}
template <int OFF> __device__ __forceinline__ bf16x8 lds_b128(unsigned addr) {
  u32x4 r; asm volatile("ds_read_b128 %0, %1 offset:%2" : "=v"(r) : "v"(addr), "i"(OFF) : "memory"); return __builtin_bit_cast(bf16x8, r);
}
__device__ __forceinline__ bf16x8 join_frag(u32x2 lo, u32x2 hi) {
  u32x4 r = __builtin_shufflevector(lo, hi, 0, 1, 2, 3); return __builtin_bit_cast(bf16x8, r);
}
#define WAIT_LGKM(n) do { asm volatile("s_waitcnt lgkmcnt(" #n ")" ::: "memory"); __builtin_amdgcn_sched_barrier(0); } while (0)
#define GLDS16(gptr, ldsptr) __builtin_amdgcn_global_load_lds((const void __attribute__((address_space(1)))*)(gptr), (void __attribute__((address_space(3)))*)(ldsptr), 16, 0, 0)

__device__ __forceinline__ float gelu_tanh_f(float x) {
  const float k0 = 0.7978845608028654f, k1 = 0.044715f;
  float u = k0 * (x + k1 * x * x * x);
  return x * __builtin_amdgcn_rcpf(1.0f + __expf(-2.0f * u));
}

constexpr int G1_BM = 256, G1_BN = 288, G1_CH = G1_BN / 8;
constexpr int G1_BK = 32, G1_NST = 4, G1_NK = 1024 / G1_BK;
constexpr int G1_A_BYTES = G1_BM * G1_BK * 2, G1_B_BYTES = G1_BK * G1_BN * 2, G1_STAGE = G1_A_BYTES + G1_B_BYTES;
__device__ __forceinline__ int g1_swz(int k) { return ((k >> 3) & 1) << 1; }
#define WAIT_VM(n) asm volatile("s_waitcnt vmcnt(" #n ") lgkmcnt(0)" ::: "memory")
#define RAW_BAR() __builtin_amdgcn_s_barrier()

__global__ __launch_bounds__(512, 2) void gemm1_kernel(
    const bf16* __restrict__ wg, const bf16* __restrict__ wu, char* __restrict__ ws) {
  extern __shared__ __attribute__((aligned(16))) char smem[];
  const int tid = threadIdx.x, lane = tid & 63, wid = __builtin_amdgcn_readfirstlane(tid >> 6);
  const int bid = blockIdx.x;
  const int xcd = bid & 7, idx = bid >> 3;
  const int pn = xcd * 4 + (idx & 3), pm = idx >> 2;
  const int brow = pm * G1_BM;
  const int wr = wid & 3, wc = wid >> 2;
  const unsigned short* xn = (const unsigned short*)(ws + OFF_XN);
  const unsigned short* bt = (const unsigned short*)(ws + OFF_BT);

  unsigned aoffs[2];
#pragma unroll
  for (int i = 0; i < 2; ++i) {
    int sb = lane * 16, swz = sb ^ (((sb >> 9) & 1) << 5);
    int R = (i * 8 + wid) * 16 + (swz >> 6), C = (swz & 63) >> 1;
    aoffs[i] = (unsigned)((brow + R) * LDXN + C);
  }
  const unsigned short* bsrc[3];
#pragma unroll
  for (int i = 0; i < 3; ++i) {
    int inst = i * 8 + wid; if (inst > 17) inst = 17;
    int Lc = inst * 64 + lane, k = Lc / G1_CH, cp = Lc % G1_CH, c = cp ^ g1_swz(k);
    int half = c & 1, P = pn * 144 + (c >> 1) * 8;
    const unsigned short* base = (const unsigned short*)(half ? wu : wg);
    int o = P;
    if (P >= FF) { base = bt; o = half * 1024 + (P - FF); }
    bsrc[i] = base + (k * LDW + o);
  }
#define G1_STAGE_LOADS(buf) do { \
    char* sa_ = smem + (buf) * G1_STAGE; char* sb_ = sa_ + G1_A_BYTES; \
    _Pragma("unroll") for (int i = 0; i < 2; ++i) { GLDS16(xn + aoffs[i], sa_ + (i * 8 + wid) * 1024); aoffs[i] += G1_BK; } \
    _Pragma("unroll") for (int i = 0; i < 2; ++i) { GLDS16(bsrc[i], sb_ + (i * 8 + wid) * 1024); bsrc[i] += G1_BK * LDW; } \
    if (wid < 2) { GLDS16(bsrc[2], sb_ + (16 + wid) * 1024); bsrc[2] += G1_BK * LDW; } \
  } while (0)
  f32x4 acc[4][9];
#pragma unroll
  for (int i = 0; i < 4; ++i)
#pragma unroll
    for (int j = 0; j < 9; ++j) acc[i][j] = (f32x4){0.f, 0.f, 0.f, 0.f};

  const int fr = lane & 15, fq = lane >> 4, tq = (lane & 15) >> 2, tp = lane & 3;
  const int ob_ = fr * 64 + fq * 16, aoff0 = (wr * 4) * 1024 + (ob_ ^ (((ob_ >> 9) & 1) << 5));
  const int bRow = G1_A_BYTES + ((8 * fq + tq) * G1_CH + (tp >> 1)) * 16 + 8 * (tp & 1) + 32 * 9 * wc;
  const int sgn = 32 * (fq & 1);
  const unsigned ldsb = (unsigned)(size_t)(__attribute__((address_space(3))) char*)smem;
  const unsigned aA = ldsb + aoff0;
  const unsigned aE = ldsb + bRow + (wc ? -sgn : sgn);
  const unsigned aO = ldsb + bRow + (wc ? sgn : -sgn);
#define G1_RDB1(jj, ad) join_frag(lds_tr64<32 * (jj)>((ad) + bo_), lds_tr64<32 * (jj) + 4 * G1_CH * 16>((ad) + bo_))
#define G1_READ(buf) do { \
    const unsigned bo_ = (buf) * G1_STAGE; \
    bfr[0] = G1_RDB1(0, aE); bfr[1] = G1_RDB1(1, aO); bfr[2] = G1_RDB1(2, aE); \
    af[0] = lds_b128<0>(aA + bo_); af[1] = lds_b128<1024>(aA + bo_); af[2] = lds_b128<2048>(aA + bo_); af[3] = lds_b128<3072>(aA + bo_); \
    bfr[3] = G1_RDB1(3, aO); bfr[4] = G1_RDB1(4, aE); bfr[5] = G1_RDB1(5, aO); \
    bfr[6] = G1_RDB1(6, aE); bfr[7] = G1_RDB1(7, aO); bfr[8] = G1_RDB1(8, aE); \
  } while (0)
#define G1_MMA() do { \
    __builtin_amdgcn_s_setprio(1); \
    _Pragma("unroll") for (int jj = 0; jj < 9; ++jj) \
      _Pragma("unroll") for (int mi = 0; mi < 4; ++mi) \
        acc[mi][jj] = __builtin_amdgcn_mfma_f32_16x16x32_bf16(af[mi], bfr[jj], acc[mi][jj], 0, 0, 0); \
    __builtin_amdgcn_s_setprio(0); \
  } while (0)
#define G1_WAITN(n2, n1) do { if (wid < 2) WAIT_VM(n2); else WAIT_VM(n1); __builtin_amdgcn_sched_barrier(0); } while (0)
#define G1_STEP(u, LOADS, w2, w1) do { \
    G1_READ(u); \
    LOADS; \
    G1_WAITN(w2, w1); RAW_BAR(); \
    G1_MMA(); \
    G1_WAITN(w2, w1); RAW_BAR(); \
  } while (0)

  bf16x8 af[4], bfr[9];
  G1_STAGE_LOADS(0); G1_STAGE_LOADS(1); G1_STAGE_LOADS(2);
  G1_WAITN(10, 8); RAW_BAR();
  if (wc == 1) RAW_BAR();
  for (int kt0 = 0; kt0 < G1_NK - 4; kt0 += 4) {
    G1_STEP(0, G1_STAGE_LOADS(3), 10, 8);
    G1_STEP(1, G1_STAGE_LOADS(0), 10, 8);
    G1_STEP(2, G1_STAGE_LOADS(1), 10, 8);
    G1_STEP(3, G1_STAGE_LOADS(2), 10, 8);
  }
  G1_STEP(0, G1_STAGE_LOADS(3), 10, 8);
  G1_STEP(1, , 5, 4);
  G1_STEP(2, , 0, 0);
  G1_STEP(3, , 0, 0);
  if (wc == 0) RAW_BAR();

  int tz = threadIdx.x; asm volatile("" : "+v"(tz));
  const int lane2 = tz & 63, fr2 = tz & 15, fq2 = (tz >> 4) & 3;
  const bool hi = fr2 >= 8;
  const int f8 = fr2 & 7, rb = fq2 * 4 + (hi ? 2 : 0);
  unsigned short* ah = (unsigned short*)(ws + OFF_AH);
  float2* bu = (float2*)(ws + OFF_BU);
  const float2* coef = (const float2*)(ws + OFF_COEF);
  const float2* lamb = (const float2*)(ws + OFF_LAM);
  float2* Eo = (float2*)(ws + OFF_E);
  const int P0 = pn * 144 + wc * 72;
  const int nf = min(9, max(0, (FF - P0) >> 3));
  RAW_BAR();
  constexpr int EP_LD = 160;
  char* wl = smem + wid * (64 * EP_LD);
#pragma unroll
  for (int jj = 0; jj < 9; ++jj) {
    if (jj < nf) {
#pragma unroll
      for (int mi = 0; mi < 4; ++mi) {
        const f32x4 v = acc[mi][jj];
        const float r0 = __shfl_xor(hi ? v[0] : v[2], 8), r1 = __shfl_xor(hi ? v[1] : v[3], 8);
        const float g0 = rbf(hi ? r0 : v[0]), g1 = rbf(hi ? r1 : v[1]);
        const float u0 = rbf(hi ? v[2] : r0), u1 = rbf(hi ? v[3] : r1);
        char* d = wl + (mi * 16 + rb) * EP_LD + (jj * 8 + f8) * 2;
        *(unsigned short*)(d) = f2bf(rbf(gelu_tanh_f(g0)) * u0);
        *(unsigned short*)(d + EP_LD) = f2bf(rbf(gelu_tanh_f(g1)) * u1);
      }
    } else {
      const int p = P0 + jj * 8 - FF + f8;
      const float2 cf = coef[p];
      const float2 lam = lamb[p];
      const float l2r = lam.x * lam.x - lam.y * lam.y, l2i = 2.f * lam.x * lam.y;
      const float l4r = l2r * l2r - l2i * l2i, l4i = 2.f * l2r * l2i;
      const float l8r = l4r * l4r - l4i * l4i, l8i = 2.f * l4r * l4i;
      const float l16r = l8r * l8r - l8i * l8i, l16i = 2.f * l8r * l8i;
      float mr = (fq2 & 1) ? 1.f : l4r, mim = (fq2 & 1) ? 0.f : l4i;
      if (!(fq2 & 2)) { const float a_ = mr * l8r - mim * l8i, b_ = mr * l8i + mim * l8r; mr = a_; mim = b_; }
      if (!hi) { const float a_ = mr * l2r - mim * l2i, b_ = mr * l2i + mim * l2r; mr = a_; mim = b_; }
      float er[4], ei[4];
#pragma unroll
      for (int mi = 0; mi < 4; ++mi) {
        const f32x4 v = acc[mi][jj];
        const float r0 = __shfl_xor(hi ? v[0] : v[2], 8), r1 = __shfl_xor(hi ? v[1] : v[3], 8);
        const float ur0 = hi ? r0 : v[0], ur1 = hi ? r1 : v[1], ui0 = hi ? v[2] : r0, ui1 = hi ? v[3] : r1;
        const float b0r = cf.x * ur0 - cf.y * ui0, b0i = cf.x * ui0 + cf.y * ur0;
        const float b1r = cf.x * ur1 - cf.y * ui1, b1i = cf.x * ui1 + cf.y * ur1;
        const int row = brow + wr * 64 + mi * 16 + rb;
        bu[(size_t)row * PP + p] = make_float2(b0r, b0i);
        bu[(size_t)(row + 1) * PP + p] = make_float2(b1r, b1i);
        er[mi] = lam.x * b0r - lam.y * b0i + b1r; ei[mi] = lam.x * b0i + lam.y * b0r + b1i;
      }
#pragma unroll
      for (int ch = 0; ch < 2; ++ch) {
        const float tr_ = l16r * er[2 * ch] - l16i * ei[2 * ch] + er[2 * ch + 1], ti_ = l16r * ei[2 * ch] + l16i * er[2 * ch] + ei[2 * ch + 1];
        float sr = mr * tr_ - mim * ti_, si = mr * ti_ + mim * tr_;
        sr += __shfl_xor(sr, 8); si += __shfl_xor(si, 8);
        sr += __shfl_xor(sr, 16); si += __shfl_xor(si, 16);
        sr += __shfl_xor(sr, 32); si += __shfl_xor(si, 32);
        if (lane2 < 8) Eo[(size_t)(((brow + wr * 64) >> 5) + ch) * PP + p] = make_float2(sr, si);
      }
    }
  }
  asm volatile("s_waitcnt lgkmcnt(0)" ::: "memory");
  {
    unsigned short* dst0 = ah + (size_t)(brow + wr * 64) * KAH + P0;
#pragma unroll
    for (int i = 0; i < 9; ++i) {
      const int q = i * 64 + lane2, row = q / 9, ch = q % 9;
      if (ch < nf) {
        bf16x8 v = *(const bf16x8*)(wl + row * EP_LD + ch * 16);
        *(bf16x8*)(dst0 + (size_t)row * KAH + ch * 8) = v;
      }
    }
  }
}

__global__ __launch_bounds__(256) void scanA_kernel(char* __restrict__ ws) {
  const int p = (blockIdx.x & 3) * 256 + threadIdx.x, c = blockIdx.x >> 2;
  const float2 lam = ((const float2*)(ws + OFF_LAM))[p];
  const float2* bu = (const float2*)(ws + OFF_BU) + (size_t)c * SCAN_T * PP + p;
  float er = 0.f, ei = 0.f;
#pragma unroll 8
  for (int s = 0; s < SCAN_T; ++s) {
    float2 b = bu[(size_t)s * PP];
    float nr = lam.x * er - lam.y * ei + b.x, ni = lam.x * ei + lam.y * er + b.y;
    er = nr; ei = ni;
  }
  ((float2*)(ws + OFF_E))[(size_t)c * PP + p] = make_float2(er, ei);
}

__global__ __launch_bounds__(256) void scanC_kernel(char* __restrict__ ws, const bf16* __restrict__ wd) {
  if (blockIdx.x >= 4 * NCHUNK) {
    const bf16x8* src = (const bf16x8*)wd + (size_t)(blockIdx.x - 4 * NCHUNK) * 1024 + threadIdx.x;
    bf16x8 v0 = src[0], v1 = src[256], v2 = src[512], v3 = src[768];
    asm volatile("" :: "v"(v0), "v"(v1), "v"(v2), "v"(v3));
    return;
  }
  const int p = (blockIdx.x & 3) * 256 + threadIdx.x, c = blockIdx.x >> 2;
  const float2* bu = (const float2*)(ws + OFF_BU) + (size_t)c * SCAN_T * PP + p;
  float2 b[SCAN_T];
#pragma unroll
  for (int s = 0; s < SCAN_T; ++s) b[s] = bu[(size_t)s * PP];
  const float2 lam = ((const float2*)(ws + OFF_LAM))[p];
  const float2* E = (const float2*)(ws + OFF_E) + p;
  float lr = lam.x, li = lam.y;
#pragma unroll
  for (int i = 0; i < 5; ++i) { float a = lr * lr - li * li, bb = 2.f * lr * li; lr = a; li = bb; }
  float hr = 0.f, hi = 0.f;
  {
    float2 e[NCHUNK - 1];
#pragma unroll
    for (int u = 0; u < NCHUNK - 1; ++u) e[u] = (u < c) ? E[(size_t)u * PP] : make_float2(0.f, 0.f);
#pragma unroll
    for (int u = 0; u < NCHUNK - 1; ++u) if (u < c) { float nr = lr * hr - li * hi + e[u].x, ni = lr * hi + li * hr + e[u].y; hr = nr; hi = ni; }
  }
  unsigned short* ah = (unsigned short*)(ws + OFF_AH) + (size_t)c * SCAN_T * KAH + FF + p;
#pragma unroll
  for (int s = 0; s < SCAN_T; ++s) {
    float nr = lam.x * hr - lam.y * hi + b[s].x, ni = lam.x * hi + lam.y * hr + b[s].y;
    hr = nr; hi = ni;
    ah[(size_t)s * KAH] = f2bf(hr);
    ah[(size_t)s * KAH + PP] = f2bf(hi);
  }
}

constexpr int G2_BM = 128, G2_BN = 64, G2_CH = G2_BN / 8;
constexpr int G2_A_BYTES = G2_BM * 64 * 2, G2_B_BYTES = 64 * G2_BN * 2, G2_STAGE = G2_A_BYTES + G2_B_BYTES;
constexpr int G2_NST = 5;
__device__ __forceinline__ int g2_swz(int k) { return (((k >> 1) & 1) << 1) | (((k >> 3) & 1) << 2); }

__global__ __launch_bounds__(256) void gemm2_kernel(
    const bf16* __restrict__ x, const bf16* __restrict__ wd, const bf16* __restrict__ dskip,
    const char* __restrict__ ws, float* __restrict__ out) {
  extern __shared__ __attribute__((aligned(16))) char smem[];
  const int tid = threadIdx.x, lane = tid & 63, wid = __builtin_amdgcn_readfirstlane(tid >> 6);
  const int bid = blockIdx.x;
  const int xcd = bid & 7, idx = bid >> 3;
  const int pm = (xcd & 3) * 4 + (idx & 3), pn = (xcd >> 2) * 8 + (idx >> 2);
  const int brow = pm * G2_BM, bcol = pn * G2_BN;
  const unsigned short* ah = (const unsigned short*)(ws + OFF_AH);
  const unsigned short* ct = (const unsigned short*)(ws + OFF_CT);
  const unsigned short* asrc[4];
#pragma unroll
  for (int i = 0; i < 4; ++i) {
    int R = (i * 4 + wid) * 8 + (lane >> 3), C = ((lane & 7) ^ (R & 7)) * 8;
    asrc[i] = ah + (size_t)(brow + R) * KAH + C;
  }
  int boffs[2];
#pragma unroll
  for (int i = 0; i < 2; ++i) {
    int Lc = (i * 4 + wid) * 64 + lane, k = Lc / G2_CH, cp = Lc % G2_CH, c = cp ^ g2_swz(k);
    boffs[i] = k * DM + bcol + c * 8;
  }
#define ABL_A(x) x
#define ABL_B(x) x
#define G2_STAGE_LOADS(buf, kt) do { \
    char* sa_ = smem + (buf) * G2_STAGE; char* sb_ = sa_ + G2_A_BYTES; \
    ABL_A(_Pragma("unroll") for (int i = 0; i < 4; ++i) GLDS16(asrc[i] + (kt) * 64, sa_ + (i * 4 + wid) * 1024);) \
    const unsigned short* bb_ = (kt) < 56 ? (const unsigned short*)wd + (size_t)(kt) * 64 * DM : ct + (size_t)((kt) - 56) * 64 * DM; \
    ABL_B(_Pragma("unroll") for (int i = 0; i < 2; ++i) GLDS16(bb_ + boffs[i], sb_ + (i * 4 + wid) * 1024);) \
  } while (0)
  const int wr = wid >> 1, wc = wid & 1;
  const int fr = lane & 15, fq = lane >> 4, tq = (lane & 15) >> 2, tp = lane & 3;
  f32x4 accd[4][2], accy[4][2];
#pragma unroll
  for (int i = 0; i < 4; ++i)
#pragma unroll
    for (int j = 0; j < 2; ++j) { accd[i][j] = (f32x4){0.f, 0.f, 0.f, 0.f}; accy[i][j] = (f32x4){0.f, 0.f, 0.f, 0.f}; }

  const unsigned ldsb = (unsigned)(size_t)(__attribute__((address_space(3))) char*)smem;
  const unsigned aA0 = ldsb + (wr * 64 + fr) * 128 + ((fq ^ (fr & 7)) * 16);
  const unsigned aA1 = ldsb + (wr * 64 + fr) * 128 + (((4 + fq) ^ (fr & 7)) * 16);
  unsigned aB[2][2];
#pragma unroll
  for (int nj = 0; nj < 2; ++nj)
#pragma unroll
    for (int t = 0; t < 2; ++t) {
      const int k0 = 8 * fq + tq + 4 * t, j = wc * 2 + nj;
      aB[nj][t] = ldsb + G2_A_BYTES + (k0 * G2_CH + ((2 * j + (tp >> 1)) ^ g2_swz(k0))) * 16 + 8 * (tp & 1);
    }
#define G2_RD(af, bfm, buf, kk) do { \
    bfm[0] = join_frag(lds_tr64<(kk) * 32 * G2_CH * 16>(aB[0][0] + (buf) * G2_STAGE), lds_tr64<(kk) * 32 * G2_CH * 16>(aB[0][1] + (buf) * G2_STAGE)); \
    bfm[1] = join_frag(lds_tr64<(kk) * 32 * G2_CH * 16>(aB[1][0] + (buf) * G2_STAGE), lds_tr64<(kk) * 32 * G2_CH * 16>(aB[1][1] + (buf) * G2_STAGE)); \
    af[0] = lds_b128<0>(((kk) ? aA1 : aA0) + (buf) * G2_STAGE); af[1] = lds_b128<2048>(((kk) ? aA1 : aA0) + (buf) * G2_STAGE); \
    af[2] = lds_b128<4096>(((kk) ? aA1 : aA0) + (buf) * G2_STAGE); af[3] = lds_b128<6144>(((kk) ? aA1 : aA0) + (buf) * G2_STAGE); \
  } while (0)
#define G2_MMA(acc, af, bfm) do { \
    _Pragma("unroll") for (int mi = 0; mi < 4; ++mi) \
      _Pragma("unroll") for (int nj = 0; nj < 2; ++nj) acc[mi][nj] = __builtin_amdgcn_mfma_f32_16x16x32_bf16(af[mi], bfm[nj], acc[mi][nj], 0, 0, 0); \
  } while (0)
#define compute(buf, acc) do { \
    bf16x8 a0[4], a1[4], f0[2], f1[2]; \
    G2_RD(a0, f0, buf, 0); \
    WAIT_LGKM(0); \
    G2_RD(a1, f1, buf, 1); \
    G2_MMA(acc, a0, f0); \
    WAIT_LGKM(0); \
    G2_MMA(acc, a1, f1); \
  } while (0)
  G2_STAGE_LOADS(0, 0); G2_STAGE_LOADS(1, 1); G2_STAGE_LOADS(2, 2); G2_STAGE_LOADS(3, 3);
#define G2_STEP(u, kt, acc) do { WAIT_VM(18); RAW_BAR(); G2_STAGE_LOADS(((u) + 4) % 5, (kt) + 4); compute(u, acc); } while (0)
  for (int kt0 = 0; kt0 < 55; kt0 += 5) {
    G2_STEP(0, kt0, accd); G2_STEP(1, kt0 + 1, accd); G2_STEP(2, kt0 + 2, accd); G2_STEP(3, kt0 + 3, accd); G2_STEP(4, kt0 + 4, accd);
  }
  WAIT_VM(18); RAW_BAR(); G2_STAGE_LOADS(4, 59); compute(0, accd);
  for (int kt0 = 56; kt0 < 81; kt0 += 5) {
    G2_STEP(1, kt0, accy); G2_STEP(2, kt0 + 1, accy); G2_STEP(3, kt0 + 2, accy); G2_STEP(4, kt0 + 3, accy); G2_STEP(0, kt0 + 4, accy);
  }
  WAIT_VM(18); RAW_BAR(); G2_STAGE_LOADS(0, 85); compute(1, accy);
  WAIT_VM(18); RAW_BAR(); G2_STAGE_LOADS(1, 86); compute(2, accy);
  WAIT_VM(18); RAW_BAR(); G2_STAGE_LOADS(2, 87); compute(3, accy);
  WAIT_VM(18); RAW_BAR(); compute(4, accy);
  WAIT_VM(12); RAW_BAR(); compute(0, accy);
  WAIT_VM(6); RAW_BAR(); compute(1, accy);
  WAIT_VM(0); RAW_BAR(); compute(2, accy);
  const unsigned short* xs = (const unsigned short*)x;
  const unsigned short* xn = (const unsigned short*)(ws + OFF_XN);
  WAIT_VM(0); RAW_BAR();
  constexpr int EP_LD = 32 * 8 + 16;
  char* wl = smem + wid * (64 * EP_LD);
#pragma unroll
  for (int nj = 0; nj < 2; ++nj)
#pragma unroll
    for (int mi = 0; mi < 4; ++mi)
#pragma unroll
      for (int r = 0; r < 4; ++r) {
        const float dn = rbf(accd[mi][nj][r]);
        const float g = rbf(dn * __builtin_amdgcn_rcpf(1.0f + __expf(-dn)));
        *(float2*)(wl + (mi * 16 + fq * 4 + r) * EP_LD + (nj * 16 + fr) * 8) = make_float2(2.0f * accy[mi][nj][r], g);
      }
  asm volatile("s_waitcnt lgkmcnt(0)" ::: "memory");
  {
    const int c4 = (lane & 7) * 4, rsub = lane >> 3;
    const int col = bcol + wc * 32 + c4;
    const unsigned short* dk = (const unsigned short*)dskip + col;
    const bf16x4 dk4 = *(const bf16x4*)dk;
#pragma unroll
    for (int i = 0; i < 8; ++i) {
      const int rl = i * 8 + rsub, row = brow + wr * 64 + rl;
      const size_t o = (size_t)row * DM + col;
      const bf16x4 x4 = *(const bf16x4*)(xs + o), n4 = *(const bf16x4*)(xn + (size_t)row * LDXN + col);
      const f32x4 p0 = *(const f32x4*)(wl + rl * EP_LD + c4 * 8), p1 = *(const f32x4*)(wl + rl * EP_LD + c4 * 8 + 16);
      f32x4 res;
      res[0] = bf2f((unsigned short)x4[0]) + (p0[0] + rbf(bf2f((unsigned short)n4[0]) * bf2f((unsigned short)dk4[0]))) * p0[1];
      res[1] = bf2f((unsigned short)x4[1]) + (p0[2] + rbf(bf2f((unsigned short)n4[1]) * bf2f((unsigned short)dk4[1]))) * p0[3];
      res[2] = bf2f((unsigned short)x4[2]) + (p1[0] + rbf(bf2f((unsigned short)n4[2]) * bf2f((unsigned short)dk4[2]))) * p1[1];
      res[3] = bf2f((unsigned short)x4[3]) + (p1[2] + rbf(bf2f((unsigned short)n4[3]) * bf2f((unsigned short)dk4[3]))) * p1[3];
      *(f32x4*)(out + o) = res;
    }
  }
}

extern "C" void kernel_launch(void* const* d_in, const int* in_sizes, int n_in,
                              void* d_out, int out_size, void* d_ws, size_t ws_size,
                              hipStream_t stream) {
  const bf16* x = (const bf16*)d_in[0];
  const bf16* lam_re = (const bf16*)d_in[1];
  const bf16* lam_im = (const bf16*)d_in[2];
  const bf16* b_re = (const bf16*)d_in[3];
  const bf16* b_im = (const bf16*)d_in[4];
  const bf16* c_re = (const bf16*)d_in[5];
  const bf16* c_im = (const bf16*)d_in[6];
  const bf16* log_dt = (const bf16*)d_in[7];
  const bf16* dskip = (const bf16*)d_in[8];
  const bf16* wg = (const bf16*)d_in[10];
  const bf16* wu = (const bf16*)d_in[11];
  const bf16* wd = (const bf16*)d_in[12];
  char* ws = (char*)d_ws;
  float* out = (float*)d_out;

  hipFuncSetAttribute((const void*)gemm1_kernel, hipFuncAttributeMaxDynamicSharedMemorySize, G1_NST * G1_STAGE);
  hipFuncSetAttribute((const void*)gemm2_kernel, hipFuncAttributeMaxDynamicSharedMemorySize, G2_NST * G2_STAGE);

  prep_kernel<<<PREP_BLOCKS, 256, 0, stream>>>(x, lam_re, lam_im, b_re, b_im, c_re, c_im, log_dt, ws, wg, wu);
  gemm1_kernel<<<256, 512, G1_NST * G1_STAGE, stream>>>(wg, wu, ws);
  scanC_kernel<<<4 * NCHUNK + 448, 256, 0, stream>>>(ws, wd);
  gemm2_kernel<<<256, 256, G2_NST * G2_STAGE, stream>>>(x, wd, dskip, ws, out);
}
```

```cpp
#include <hip/hip_runtime.h>
#include <hip/hip_bf16.h>
#include <stdint.h>

typedef __hip_bfloat16 bf16;
typedef __attribute__((ext_vector_type(8))) short bf16x8;
typedef __attribute__((ext_vector_type(4))) short bf16x4;
typedef __attribute__((ext_vector_type(4))) float f32x4;
typedef __attribute__((__vector_size__(4 * sizeof(short)))) short v4s;

constexpr int L_SEQ = 2048, DM = 1024, FF = 3584, PP = 1024;
constexpr int LDW = 3584;
constexpr int LDXN = DM + 64;
constexpr int KAH = FF + 2 * PP + 64;
constexpr int SCAN_T = 32, NCHUNK = L_SEQ / SCAN_T;

constexpr size_t OFF_XN   = 0;
constexpr size_t OFF_BT   = OFF_XN + (size_t)L_SEQ * LDXN * 2;
constexpr size_t OFF_CT   = OFF_BT + (size_t)DM * LDW * 2;
constexpr size_t OFF_AH   = OFF_CT + (size_t)2 * PP * DM * 2;
constexpr size_t OFF_BU   = OFF_AH + (size_t)L_SEQ * KAH * 2;
constexpr size_t OFF_E    = OFF_BU + (size_t)L_SEQ * PP * 8;
constexpr size_t OFF_LAM  = OFF_E + (size_t)NCHUNK * PP * 8;
constexpr size_t OFF_COEF = OFF_LAM + (size_t)PP * 8;

__device__ __forceinline__ float bf2f(unsigned short u) { return __uint_as_float(((unsigned)u) << 16); }
__device__ __forceinline__ unsigned short f2bf(float f) {
  __bf16 b = (__bf16)f;
  return __builtin_bit_cast(unsigned short, b);
}
__device__ __forceinline__ float rbf(float f) { return bf2f(f2bf(f)); }

constexpr int PREP_LN_BLOCKS = L_SEQ / 4;
constexpr int PREP_TR_BLOCKS = 4 * (1024 / 64) * (1024 / 64);
constexpr int PREP_PF_BLOCKS = 448;
constexpr int PREP_BLOCKS = PREP_LN_BLOCKS + PREP_TR_BLOCKS + 4 + PREP_PF_BLOCKS;

__global__ __launch_bounds__(256) void prep_kernel(
    const bf16* __restrict__ x, const bf16* __restrict__ lam_re, const bf16* __restrict__ lam_im,
    const bf16* __restrict__ b_re, const bf16* __restrict__ b_im,
    const bf16* __restrict__ c_re, const bf16* __restrict__ c_im,
    const bf16* __restrict__ log_dt, char* __restrict__ ws,
    const bf16* __restrict__ wg, const bf16* __restrict__ wu) {
  __shared__ unsigned short tile[64][66];
  const int tid = threadIdx.x;
  if (blockIdx.x < PREP_PF_BLOCKS) {
    const int m = blockIdx.x / 224, part = blockIdx.x % 224;
    const bf16x8* src = (const bf16x8*)(m == 0 ? wg : wu) + (size_t)part * 2048 + tid;
    bf16x8 v[8];
#pragma unroll
    for (int i = 0; i < 8; ++i) v[i] = src[i * 256];
    asm volatile("" :: "v"(v[0]), "v"(v[1]), "v"(v[2]), "v"(v[3]), "v"(v[4]), "v"(v[5]), "v"(v[6]), "v"(v[7]));
    return;
  }
  const int bid = blockIdx.x - PREP_PF_BLOCKS;
  if (bid < PREP_LN_BLOCKS) {
    const int wave = tid >> 6, lane = tid & 63;
    const int row = bid * 4 + wave;
    const unsigned short* xr = (const unsigned short*)x + (size_t)row * DM;
    float v[16];
    bf16x8 a = *(const bf16x8*)(xr + lane * 8);
    bf16x8 b = *(const bf16x8*)(xr + 512 + lane * 8);
    float sum = 0.f;
#pragma unroll
    for (int j = 0; j < 8; ++j) { v[j] = bf2f((unsigned short)a[j]); v[8 + j] = bf2f((unsigned short)b[j]); }
#pragma unroll
    for (int j = 0; j < 16; ++j) sum += v[j];
#pragma unroll
    for (int o = 32; o > 0; o >>= 1) sum += __shfl_xor(sum, o);
    const float mean = sum * (1.0f / DM);
    float sq = 0.f;
#pragma unroll
    for (int j = 0; j < 16; ++j) { float d = v[j] - mean; sq += d * d; }
#pragma unroll
    for (int o = 32; o > 0; o >>= 1) sq += __shfl_xor(sq, o);
    const float var_b = rbf(sq * (1.0f / DM));
    const float mean_b = rbf(mean);
    const float den = rbf(sqrtf(rbf(var_b + 1e-5f)));
    bf16x8 oa, ob;
#pragma unroll
    for (int j = 0; j < 8; ++j) {
      oa[j] = (short)f2bf(rbf(v[j] - mean_b) / den);
      ob[j] = (short)f2bf(rbf(v[8 + j] - mean_b) / den);
    }
    unsigned short* xo = (unsigned short*)(ws + OFF_XN) + (size_t)row * LDXN;
    *(bf16x8*)(xo + lane * 8) = oa;
    *(bf16x8*)(xo + 512 + lane * 8) = ob;
  } else if (bid < PREP_LN_BLOCKS + PREP_TR_BLOCKS) {
    const int t = bid - PREP_LN_BLOCKS;
    const int mat = t >> 8, ti = (t >> 4) & 15, tj = t & 15;
    const unsigned short* src = (const unsigned short*)(mat == 0 ? b_re : mat == 1 ? b_im : mat == 2 ? c_re : c_im);
    const int tc = tid & 7, tr = tid >> 3;
#pragma unroll
    for (int i = 0; i < 2; ++i) {
      const int r = tr + 32 * i;
      bf16x8 v = *(const bf16x8*)(src + (size_t)(ti * 64 + r) * 1024 + tj * 64 + tc * 8);
#pragma unroll
      for (int e = 0; e < 8; ++e) tile[r][tc * 8 + e] = (unsigned short)v[e];
    }
    __syncthreads();
    unsigned short* dst; size_t ld; unsigned short neg = 0;
    if (mat < 2) { dst = (unsigned short*)(ws + OFF_BT) + mat * 1024; ld = LDW; }
    else { dst = (unsigned short*)(ws + OFF_CT) + (size_t)(mat - 2) * 1024 * DM; ld = DM; neg = (mat == 3) ? 0x8000 : 0; }
#pragma unroll
    for (int i = 0; i < 2; ++i) {
      const int c = tr + 32 * i;
      bf16x8 v;
#pragma unroll
      for (int e = 0; e < 8; ++e) v[e] = (short)(tile[tc * 8 + e][c] ^ neg);
      *(bf16x8*)(dst + (size_t)(tj * 64 + c) * ld + ti * 64 + tc * 8) = v;
    }
  } else {
    const int p = (bid - PREP_LN_BLOCKS - PREP_TR_BLOCKS) * 256 + tid;
    const float lr = bf2f(((const unsigned short*)lam_re)[p]);
    const float li = bf2f(((const unsigned short*)lam_im)[p]);
    const float dt = rbf(expf(bf2f(((const unsigned short*)log_dt)[p])));
    const float ar = dt * lr, ai = dt * li;
    const float e = expf(ar);
    float sn, cs; sincosf(ai, &sn, &cs);
    const float br_ = e * cs, bi_ = e * sn;
    const float nr = br_ - 1.0f, ni = bi_;
    const float den = lr * lr + li * li;
    const float cr = (nr * lr + ni * li) / den, ci = (ni * lr - nr * li) / den;
    ((float2*)(ws + OFF_LAM))[p] = make_float2(br_, bi_);
    ((float2*)(ws + OFF_COEF))[p] = make_float2(cr, ci);
  }
}

__device__ __forceinline__ int a_lds_byte(int r, int c) {
  int st = (r >> 4) * 2 + (c >> 5), rr = r & 15, cc = c & 31, ob = rr * 64 + cc * 2;
  return st * 1024 + (ob ^ (((ob >> 9) & 1) << 5));
}
__device__ __forceinline__ void a_stage_rc(int b, int& R, int& C) {
  int st = b >> 10, sb = b & 1023, swz = sb ^ (((sb >> 9) & 1) << 5);
  R = (st >> 1) * 16 + (swz >> 6); C = (st & 1) * 32 + ((swz & 63) >> 1);
}
__device__ __forceinline__ bf16x8 tr_frag(const char* lds_lo, const char* lds_hi) {
  v4s lo = __builtin_amdgcn_ds_read_tr16_b64_v4i16((__attribute__((address_space(3))) v4s*)(lds_lo));
  v4s hi = __builtin_amdgcn_ds_read_tr16_b64_v4i16((__attribute__((address_space(3))) v4s*)(lds_hi));
  bf16x8 r;
  r[0] = lo[0]; r[1] = lo[1]; r[2] = lo[2]; r[3] = lo[3];
  r[4] = hi[0]; r[5] = hi[1]; r[6] = hi[2]; r[7] = hi[3];
  return r;
}
typedef __attribute__((ext_vector_type(2))) unsigned u32x2;
typedef __attribute__((ext_vector_type(4))) unsigned u32x4;
template <int OFF> __device__ __forceinline__ u32x2 lds_tr64(unsigned addr) {
  u32x2 r; asm volatile("ds_read_b64_tr_b16 %0, %1 offset:%2" : "=v"(r) : "v"(addr), "i"(OFF) : "memory"); return r;
}
template <int OFF> __device__ __forceinline__ bf16x8 lds_b128(unsigned addr) {
  u32x4 r; asm volatile("ds_read_b128 %0, %1 offset:%2" : "=v"(r) : "v"(addr), "i"(OFF) : "memory"); return __builtin_bit_cast(bf16x8, r);
}
__device__ __forceinline__ bf16x8 join_frag(u32x2 lo, u32x2 hi) {
  u32x4 r = __builtin_shufflevector(lo, hi, 0, 1, 2, 3); return __builtin_bit_cast(bf16x8, r);
}
#define WAIT_LGKM(n) do { asm volatile("s_waitcnt lgkmcnt(" #n ")" ::: "memory"); __builtin_amdgcn_sched_barrier(0); } while (0)
#define GLDS16(gptr, ldsptr) __builtin_amdgcn_global_load_lds((const void __attribute__((address_space(1)))*)(gptr), (void __attribute__((address_space(3)))*)(ldsptr), 16, 0, 0)

__device__ __forceinline__ float gelu_tanh_f(float x) {
  const float k0 = 0.7978845608028654f, k1 = 0.044715f;
  float u = k0 * (x + k1 * x * x * x);
  return x * __builtin_amdgcn_rcpf(1.0f + __expf(-2.0f * u));
}

constexpr int G1_BM = 256, G1_BN = 288, G1_CH = G1_BN / 8;
constexpr int G1_BK = 32, G1_NST = 4, G1_NK = 1024 / G1_BK;
constexpr int G1_A_BYTES = G1_BM * G1_BK * 2, G1_B_BYTES = G1_BK * G1_BN * 2, G1_STAGE = G1_A_BYTES + G1_B_BYTES;
__device__ __forceinline__ int g1_swz(int k) { return ((k >> 3) & 1) << 1; }
#define WAIT_VM(n) asm volatile("s_waitcnt vmcnt(" #n ") lgkmcnt(0)" ::: "memory")
#define RAW_BAR() __builtin_amdgcn_s_barrier()

__global__ __launch_bounds__(512, 2) void gemm1_kernel(
    const bf16* __restrict__ wg, const bf16* __restrict__ wu, char* __restrict__ ws) {
  extern __shared__ __attribute__((aligned(16))) char smem[];
  const int tid = threadIdx.x, lane = tid & 63, wid = __builtin_amdgcn_readfirstlane(tid >> 6);
  const int bid = blockIdx.x;
  const int xcd = bid & 7, idx = bid >> 3;
  const int pn = xcd * 4 + (idx & 3), pm = idx >> 2;
  const int brow = pm * G1_BM;
  const int wr = wid & 3, wc = wid >> 2;
  const unsigned short* xn = (const unsigned short*)(ws + OFF_XN);
  const unsigned short* bt = (const unsigned short*)(ws + OFF_BT);

  unsigned aoffs[2];
#pragma unroll
  for (int i = 0; i < 2; ++i) {
    int sb = lane * 16, swz = sb ^ (((sb >> 9) & 1) << 5);
    int R = (i * 8 + wid) * 16 + (swz >> 6), C = (swz & 63) >> 1;
    aoffs[i] = (unsigned)((brow + R) * LDXN + C);
  }
  const unsigned short* bsrc[3];
#pragma unroll
  for (int i = 0; i < 3; ++i) {
    int inst = i * 8 + wid; if (inst > 17) inst = 17;
    int Lc = inst * 64 + lane, k = Lc / G1_CH, cp = Lc % G1_CH, c = cp ^ g1_swz(k);
    int half = c & 1, blk = c >> 1, w_ = blk >= 9, jj = blk - w_ * 9;
    const unsigned short* base = (const unsigned short*)(half ? wu : wg);
    int o = pn * 112 + w_ * 56 + jj * 8;
    if (jj >= 7) { base = bt; o = half * 1024 + pn * 32 + w_ * 16 + (jj - 7) * 8; }
    bsrc[i] = base + (k * LDW + o);
  }
#define G1_STAGE_LOADS(buf) do { \
    char* sa_ = smem + (buf) * G1_STAGE; char* sb_ = sa_ + G1_A_BYTES; \
    _Pragma("unroll") for (int i = 0; i < 2; ++i) { GLDS16(xn + aoffs[i], sa_ + (i * 8 + wid) * 1024); aoffs[i] += G1_BK; } \
    _Pragma("unroll") for (int i = 0; i < 2; ++i) { GLDS16(bsrc[i], sb_ + (i * 8 + wid) * 1024); bsrc[i] += G1_BK * LDW; } \
    if (wid < 2) { GLDS16(bsrc[2], sb_ + (16 + wid) * 1024); bsrc[2] += G1_BK * LDW; } \
  } while (0)
  f32x4 acc[4][9];
#pragma unroll
  for (int i = 0; i < 4; ++i)
#pragma unroll
    for (int j = 0; j < 9; ++j) acc[i][j] = (f32x4){0.f, 0.f, 0.f, 0.f};

  const int fr = lane & 15, fq = lane >> 4, tq = (lane & 15) >> 2, tp = lane & 3;
  const int ob_ = fr * 64 + fq * 16, aoff0 = (wr * 4) * 1024 + (ob_ ^ (((ob_ >> 9) & 1) << 5));
  const int bRow = G1_A_BYTES + ((8 * fq + tq) * G1_CH + (tp >> 1)) * 16 + 8 * (tp & 1) + 32 * 9 * wc;
  const int sgn = 32 * (fq & 1);
  const unsigned ldsb = (unsigned)(size_t)(__attribute__((address_space(3))) char*)smem;
  const unsigned aA = ldsb + aoff0;
  const unsigned aE = ldsb + bRow + (wc ? -sgn : sgn);
  const unsigned aO = ldsb + bRow + (wc ? sgn : -sgn);
#define G1_RDB1(jj, ad) join_frag(lds_tr64<32 * (jj)>((ad) + bo_), lds_tr64<32 * (jj) + 4 * G1_CH * 16>((ad) + bo_))
#define G1_READ(buf) do { \
    const unsigned bo_ = (buf) * G1_STAGE; \
    bfr[0] = G1_RDB1(0, aE); bfr[1] = G1_RDB1(1, aO); bfr[2] = G1_RDB1(2, aE); \
    af[0] = lds_b128<0>(aA + bo_); af[1] = lds_b128<1024>(aA + bo_); af[2] = lds_b128<2048>(aA + bo_); af[3] = lds_b128<3072>(aA + bo_); \
    bfr[3] = G1_RDB1(3, aO); bfr[4] = G1_RDB1(4, aE); bfr[5] = G1_RDB1(5, aO); \
    bfr[6] = G1_RDB1(6, aE); bfr[7] = G1_RDB1(7, aO); bfr[8] = G1_RDB1(8, aE); \
  } while (0)
#define G1_MMA() do { \
    __builtin_amdgcn_s_setprio(1); \
    _Pragma("unroll") for (int jj = 0; jj < 9; ++jj) \
      _Pragma("unroll") for (int mi = 0; mi < 4; ++mi) \
        acc[mi][jj] = __builtin_amdgcn_mfma_f32_16x16x32_bf16(af[mi], bfr[jj], acc[mi][jj], 0, 0, 0); \
    __builtin_amdgcn_s_setprio(0); \
  } while (0)
#define G1_WAITN(n2, n1) do { if (wid < 2) WAIT_VM(n2); else WAIT_VM(n1); __builtin_amdgcn_sched_barrier(0); } while (0)
#define G1_STEP(u, LOADS, w2, w1) do { \
    G1_READ(u); \
    LOADS; \
    G1_WAITN(w2, w1); RAW_BAR(); \
    G1_MMA(); \
    G1_WAITN(w2, w1); RAW_BAR(); \
  } while (0)

  bf16x8 af[4], bfr[9];
  G1_STAGE_LOADS(0); G1_STAGE_LOADS(1); G1_STAGE_LOADS(2);
  G1_WAITN(10, 8); RAW_BAR();
  if (wc == 1) RAW_BAR();
  for (int kt0 = 0; kt0 < G1_NK - 4; kt0 += 4) {
    G1_STEP(0, G1_STAGE_LOADS(3), 10, 8);
    G1_STEP(1, G1_STAGE_LOADS(0), 10, 8);
    G1_STEP(2, G1_STAGE_LOADS(1), 10, 8);
    G1_STEP(3, G1_STAGE_LOADS(2), 10, 8);
  }
  G1_STEP(0, G1_STAGE_LOADS(3), 10, 8);
  G1_STEP(1, , 5, 4);
  G1_STEP(2, , 0, 0);
  G1_STEP(3, , 0, 0);
  if (wc == 0) RAW_BAR();

  int tz = threadIdx.x; asm volatile("" : "+v"(tz));
  const int lane2 = tz & 63, fr2 = tz & 15, fq2 = (tz >> 4) & 3;
  const bool hi = fr2 >= 8;
  const int f8 = fr2 & 7, rb = fq2 * 4 + (hi ? 2 : 0);
  unsigned short* ah = (unsigned short*)(ws + OFF_AH);
  float2* bu = (float2*)(ws + OFF_BU);
  const float2* coef = (const float2*)(ws + OFF_COEF);
  const float2* lamb = (const float2*)(ws + OFF_LAM);
  float2* Eo = (float2*)(ws + OFF_E);
  const int P0 = pn * 112 + wc * 56;
  const int S0 = pn * 32 + wc * 16;
  constexpr int nf = 7;
  RAW_BAR();
  constexpr int EP_LD = 160;
  char* wl = smem + wid * (64 * EP_LD);
#pragma unroll
  for (int jj = 0; jj < 9; ++jj) {
    if (jj < nf) {
#pragma unroll
      for (int mi = 0; mi < 4; ++mi) {
        const f32x4 v = acc[mi][jj];
        const float r0 = __shfl_xor(hi ? v[0] : v[2], 8), r1 = __shfl_xor(hi ? v[1] : v[3], 8);
        const float g0 = rbf(hi ? r0 : v[0]), g1 = rbf(hi ? r1 : v[1]);
        const float u0 = rbf(hi ? v[2] : r0), u1 = rbf(hi ? v[3] : r1);
        char* d = wl + (mi * 16 + rb) * EP_LD + (jj * 8 + f8) * 2;
        *(unsigned short*)(d) = f2bf(rbf(gelu_tanh_f(g0)) * u0);
        *(unsigned short*)(d + EP_LD) = f2bf(rbf(gelu_tanh_f(g1)) * u1);
      }
    } else {
      const int p = S0 + (jj - 7) * 8 + f8;
      const float2 cf = coef[p];
      const float2 lam = lamb[p];
      const float l2r = lam.x * lam.x - lam.y * lam.y, l2i = 2.f * lam.x * lam.y;
      const float l4r = l2r * l2r - l2i * l2i, l4i = 2.f * l2r * l2i;
      const float l8r = l4r * l4r - l4i * l4i, l8i = 2.f * l4r * l4i;
      const float l16r = l8r * l8r - l8i * l8i, l16i = 2.f * l8r * l8i;
      float mr = (fq2 & 1) ? 1.f : l4r, mim = (fq2 & 1) ? 0.f : l4i;
      if (!(fq2 & 2)) { const float a_ = mr * l8r - mim * l8i, b_ = mr * l8i + mim * l8r; mr = a_; mim = b_; }
      if (!hi) { const float a_ = mr * l2r - mim * l2i, b_ = mr * l2i + mim * l2r; mr = a_; mim = b_; }
      float er[4], ei[4];
#pragma unroll
      for (int mi = 0; mi < 4; ++mi) {
        const f32x4 v = acc[mi][jj];
        const float r0 = __shfl_xor(hi ? v[0] : v[2], 8), r1 = __shfl_xor(hi ? v[1] : v[3], 8);
        const float ur0 = hi ? r0 : v[0], ur1 = hi ? r1 : v[1], ui0 = hi ? v[2] : r0, ui1 = hi ? v[3] : r1;
        const float b0r = cf.x * ur0 - cf.y * ui0, b0i = cf.x * ui0 + cf.y * ur0;
        const float b1r = cf.x * ur1 - cf.y * ui1, b1i = cf.x * ui1 + cf.y * ur1;
        const int row = brow + wr * 64 + mi * 16 + rb;
        bu[(size_t)row * PP + p] = make_float2(b0r, b0i);
        bu[(size_t)(row + 1) * PP + p] = make_float2(b1r, b1i);
        er[mi] = lam.x * b0r - lam.y * b0i + b1r; ei[mi] = lam.x * b0i + lam.y * b0r + b1i;
      }
#pragma unroll
      for (int ch = 0; ch < 2; ++ch) {
        const float tr_ = l16r * er[2 * ch] - l16i * ei[2 * ch] + er[2 * ch + 1], ti_ = l16r * ei[2 * ch] + l16i * er[2 * ch] + ei[2 * ch + 1];
        float sr = mr * tr_ - mim * ti_, si = mr * ti_ + mim * tr_;
        sr += __shfl_xor(sr, 8); si += __shfl_xor(si, 8);
        sr += __shfl_xor(sr, 16); si += __shfl_xor(si, 16);
        sr += __shfl_xor(sr, 32); si += __shfl_xor(si, 32);
        if (lane2 < 8) Eo[(size_t)(((brow + wr * 64) >> 5) + ch) * PP + p] = make_float2(sr, si);
      }
    }
  }
  asm volatile("s_waitcnt lgkmcnt(0)" ::: "memory");
  {
    unsigned short* dst0 = ah + (size_t)(brow + wr * 64) * KAH + P0;
#pragma unroll
    for (int i = 0; i < 7; ++i) {
      const int q = i * 64 + lane2, row = q / 7, ch = q % 7;
      {
        bf16x8 v = *(const bf16x8*)(wl + row * EP_LD + ch * 16);
        *(bf16x8*)(dst0 + (size_t)row * KAH + ch * 8) = v;
      }
    }
  }
}

__global__ __launch_bounds__(256) void scanA_kernel(char* __restrict__ ws) {
  const int p = (blockIdx.x & 3) * 256 + threadIdx.x, c = blockIdx.x >> 2;
  const float2 lam = ((const float2*)(ws + OFF_LAM))[p];
  const float2* bu = (const float2*)(ws + OFF_BU) + (size_t)c * SCAN_T * PP + p;
  float er = 0.f, ei = 0.f;
#pragma unroll 8
  for (int s = 0; s < SCAN_T; ++s) {
    float2 b = bu[(size_t)s * PP];
    float nr = lam.x * er - lam.y * ei + b.x, ni = lam.x * ei + lam.y * er + b.y;
    er = nr; ei = ni;
  }
  ((float2*)(ws + OFF_E))[(size_t)c * PP + p] = make_float2(er, ei);
}

__global__ __launch_bounds__(256) void scanC_kernel(char* __restrict__ ws, const bf16* __restrict__ wd) {
  if (blockIdx.x >= 4 * NCHUNK) {
    const bf16x8* src = (const bf16x8*)wd + (size_t)(blockIdx.x - 4 * NCHUNK) * 1024 + threadIdx.x;
    bf16x8 v0 = src[0], v1 = src[256], v2 = src[512], v3 = src[768];
    asm volatile("" :: "v"(v0), "v"(v1), "v"(v2), "v"(v3));
    return;
  }
  const int p = (blockIdx.x & 3) * 256 + threadIdx.x, c = blockIdx.x >> 2;
  const float2* bu = (const float2*)(ws + OFF_BU) + (size_t)c * SCAN_T * PP + p;
  float2 b[SCAN_T];
#pragma unroll
  for (int s = 0; s < SCAN_T; ++s) b[s] = bu[(size_t)s * PP];
  const float2 lam = ((const float2*)(ws + OFF_LAM))[p];
  const float2* E = (const float2*)(ws + OFF_E) + p;
  float lr = lam.x, li = lam.y;
#pragma unroll
  for (int i = 0; i < 5; ++i) { float a = lr * lr - li * li, bb = 2.f * lr * li; lr = a; li = bb; }
  float hr = 0.f, hi = 0.f;
  {
    float2 e[NCHUNK - 1];
#pragma unroll
    for (int u = 0; u < NCHUNK - 1; ++u) e[u] = (u < c) ? E[(size_t)u * PP] : make_float2(0.f, 0.f);
#pragma unroll
    for (int u = 0; u < NCHUNK - 1; ++u) if (u < c) { float nr = lr * hr - li * hi + e[u].x, ni = lr * hi + li * hr + e[u].y; hr = nr; hi = ni; }
  }
  unsigned short* ah = (unsigned short*)(ws + OFF_AH) + (size_t)c * SCAN_T * KAH + FF + p;
#pragma unroll
  for (int s = 0; s < SCAN_T; ++s) {
    float nr = lam.x * hr - lam.y * hi + b[s].x, ni = lam.x * hi + lam.y * hr + b[s].y;
    hr = nr; hi = ni;
    ah[(size_t)s * KAH] = f2bf(hr);
    ah[(size_t)s * KAH + PP] = f2bf(hi);
  }
}

constexpr int G2_BM = 128, G2_BN = 64, G2_CH = G2_BN / 8;
constexpr int G2_A_BYTES = G2_BM * 64 * 2, G2_B_BYTES = 64 * G2_BN * 2, G2_STAGE = G2_A_BYTES + G2_B_BYTES;
constexpr int G2_NST = 5;
__device__ __forceinline__ int g2_swz(int k) { return (((k >> 1) & 1) << 1) | (((k >> 3) & 1) << 2); }

__global__ __launch_bounds__(256) void gemm2_kernel(
    const bf16* __restrict__ x, const bf16* __restrict__ wd, const bf16* __restrict__ dskip,
    const char* __restrict__ ws, float* __restrict__ out) {
  extern __shared__ __attribute__((aligned(16))) char smem[];
  const int tid = threadIdx.x, lane = tid & 63, wid = __builtin_amdgcn_readfirstlane(tid >> 6);
  const int bid = blockIdx.x;
  const int xcd = bid & 7, idx = bid >> 3;
  const int pm = (xcd & 3) * 4 + (idx & 3), pn = (xcd >> 2) * 8 + (idx >> 2);
  const int brow = pm * G2_BM, bcol = pn * G2_BN;
  const unsigned short* ah = (const unsigned short*)(ws + OFF_AH);
  const unsigned short* ct = (const unsigned short*)(ws + OFF_CT);
  const unsigned short* asrc[4];
#pragma unroll
  for (int i = 0; i < 4; ++i) {
    int R = (i * 4 + wid) * 8 + (lane >> 3), C = ((lane & 7) ^ (R & 7)) * 8;
    asrc[i] = ah + (size_t)(brow + R) * KAH + C;
  }
  int boffs[2];
#pragma unroll
  for (int i = 0; i < 2; ++i) {
    int Lc = (i * 4 + wid) * 64 + lane, k = Lc / G2_CH, cp = Lc % G2_CH, c = cp ^ g2_swz(k);
    boffs[i] = k * DM + bcol + c * 8;
  }
#define ABL_A(x) x
#define ABL_B(x) x
#define G2_STAGE_LOADS(buf, kt) do { \
    char* sa_ = smem + (buf) * G2_STAGE; char* sb_ = sa_ + G2_A_BYTES; \
    ABL_A(_Pragma("unroll") for (int i = 0; i < 4; ++i) GLDS16(asrc[i] + (kt) * 64, sa_ + (i * 4 + wid) * 1024);) \
    const unsigned short* bb_ = (kt) < 56 ? (const unsigned short*)wd + (size_t)(kt) * 64 * DM : ct + (size_t)((kt) - 56) * 64 * DM; \
    ABL_B(_Pragma("unroll") for (int i = 0; i < 2; ++i) GLDS16(bb_ + boffs[i], sb_ + (i * 4 + wid) * 1024);) \
  } while (0)
  const int wr = wid >> 1, wc = wid & 1;
  const int fr = lane & 15, fq = lane >> 4, tq = (lane & 15) >> 2, tp = lane & 3;
  f32x4 accd[4][2], accy[4][2];
#pragma unroll
  for (int i = 0; i < 4; ++i)
#pragma unroll
    for (int j = 0; j < 2; ++j) { accd[i][j] = (f32x4){0.f, 0.f, 0.f, 0.f}; accy[i][j] = (f32x4){0.f, 0.f, 0.f, 0.f}; }

  const unsigned ldsb = (unsigned)(size_t)(__attribute__((address_space(3))) char*)smem;
  const unsigned aA0 = ldsb + (wr * 64 + fr) * 128 + ((fq ^ (fr & 7)) * 16);
  const unsigned aA1 = ldsb + (wr * 64 + fr) * 128 + (((4 + fq) ^ (fr & 7)) * 16);
  unsigned aB[2][2];
#pragma unroll
  for (int nj = 0; nj < 2; ++nj)
#pragma unroll
    for (int t = 0; t < 2; ++t) {
      const int k0 = 8 * fq + tq + 4 * t, j = wc * 2 + nj;
      aB[nj][t] = ldsb + G2_A_BYTES + (k0 * G2_CH + ((2 * j + (tp >> 1)) ^ g2_swz(k0))) * 16 + 8 * (tp & 1);
    }
#define G2_RD(af, bfm, buf, kk) do { \
    bfm[0] = join_frag(lds_tr64<(kk) * 32 * G2_CH * 16>(aB[0][0] + (buf) * G2_STAGE), lds_tr64<(kk) * 32 * G2_CH * 16>(aB[0][1] + (buf) * G2_STAGE)); \
    bfm[1] = join_frag(lds_tr64<(kk) * 32 * G2_CH * 16>(aB[1][0] + (buf) * G2_STAGE), lds_tr64<(kk) * 32 * G2_CH * 16>(aB[1][1] + (buf) * G2_STAGE)); \
    af[0] = lds_b128<0>(((kk) ? aA1 : aA0) + (buf) * G2_STAGE); af[1] = lds_b128<2048>(((kk) ? aA1 : aA0) + (buf) * G2_STAGE); \
    af[2] = lds_b128<4096>(((kk) ? aA1 : aA0) + (buf) * G2_STAGE); af[3] = lds_b128<6144>(((kk) ? aA1 : aA0) + (buf) * G2_STAGE); \
  } while (0)
#define G2_MMA(acc, af, bfm) do { \
    _Pragma("unroll") for (int mi = 0; mi < 4; ++mi) \
      _Pragma("unroll") for (int nj = 0; nj < 2; ++nj) acc[mi][nj] = __builtin_amdgcn_mfma_f32_16x16x32_bf16(af[mi], bfm[nj], acc[mi][nj], 0, 0, 0); \
  } while (0)
#define compute(buf, acc) do { \
    bf16x8 a0[4], a1[4], f0[2], f1[2]; \
    G2_RD(a0, f0, buf, 0); \
    WAIT_LGKM(0); \
    G2_RD(a1, f1, buf, 1); \
    G2_MMA(acc, a0, f0); \
    WAIT_LGKM(0); \
    G2_MMA(acc, a1, f1); \
  } while (0)
  G2_STAGE_LOADS(0, 0); G2_STAGE_LOADS(1, 1); G2_STAGE_LOADS(2, 2); G2_STAGE_LOADS(3, 3);
#define G2_STEP(u, kt, acc) do { WAIT_VM(18); RAW_BAR(); G2_STAGE_LOADS(((u) + 4) % 5, (kt) + 4); compute(u, acc); } while (0)
  for (int kt0 = 0; kt0 < 55; kt0 += 5) {
    G2_STEP(0, kt0, accd); G2_STEP(1, kt0 + 1, accd); G2_STEP(2, kt0 + 2, accd); G2_STEP(3, kt0 + 3, accd); G2_STEP(4, kt0 + 4, accd);
  }
  WAIT_VM(18); RAW_BAR(); G2_STAGE_LOADS(4, 59); compute(0, accd);
  for (int kt0 = 56; kt0 < 81; kt0 += 5) {
    G2_STEP(1, kt0, accy); G2_STEP(2, kt0 + 1, accy); G2_STEP(3, kt0 + 2, accy); G2_STEP(4, kt0 + 3, accy); G2_STEP(0, kt0 + 4, accy);
  }
  WAIT_VM(18); RAW_BAR(); G2_STAGE_LOADS(0, 85); compute(1, accy);
  WAIT_VM(18); RAW_BAR(); G2_STAGE_LOADS(1, 86); compute(2, accy);
  WAIT_VM(18); RAW_BAR(); G2_STAGE_LOADS(2, 87); compute(3, accy);
  WAIT_VM(18); RAW_BAR(); compute(4, accy);
  WAIT_VM(12); RAW_BAR(); compute(0, accy);
  WAIT_VM(6); RAW_BAR(); compute(1, accy);
  WAIT_VM(0); RAW_BAR(); compute(2, accy);
  const unsigned short* xs = (const unsigned short*)x;
  const unsigned short* xn = (const unsigned short*)(ws + OFF_XN);
  WAIT_VM(0); RAW_BAR();
  constexpr int EP_LD = 32 * 8 + 16;
  char* wl = smem + wid * (64 * EP_LD);
#pragma unroll
  for (int nj = 0; nj < 2; ++nj)
#pragma unroll
    for (int mi = 0; mi < 4; ++mi)
#pragma unroll
      for (int r = 0; r < 4; ++r) {
        const float dn = rbf(accd[mi][nj][r]);
        const float g = rbf(dn * __builtin_amdgcn_rcpf(1.0f + __expf(-dn)));
        *(float2*)(wl + (mi * 16 + fq * 4 + r) * EP_LD + (nj * 16 + fr) * 8) = make_float2(2.0f * accy[mi][nj][r], g);
      }
  asm volatile("s_waitcnt lgkmcnt(0)" ::: "memory");
  {
    const int c4 = (lane & 7) * 4, rsub = lane >> 3;
    const int col = bcol + wc * 32 + c4;
    const unsigned short* dk = (const unsigned short*)dskip + col;
    const bf16x4 dk4 = *(const bf16x4*)dk;
#pragma unroll
    for (int i = 0; i < 8; ++i) {
      const int rl = i * 8 + rsub, row = brow + wr * 64 + rl;
      const size_t o = (size_t)row * DM + col;
      const bf16x4 x4 = *(const bf16x4*)(xs + o), n4 = *(const bf16x4*)(xn + (size_t)row * LDXN + col);
      const f32x4 p0 = *(const f32x4*)(wl + rl * EP_LD + c4 * 8), p1 = *(const f32x4*)(wl + rl * EP_LD + c4 * 8 + 16);
      f32x4 res;
      res[0] = bf2f((unsigned short)x4[0]) + (p0[0] + rbf(bf2f((unsigned short)n4[0]) * bf2f((unsigned short)dk4[0]))) * p0[1];
      res[1] = bf2f((unsigned short)x4[1]) + (p0[2] + rbf(bf2f((unsigned short)n4[1]) * bf2f((unsigned short)dk4[1]))) * p0[3];
      res[2] = bf2f((unsigned short)x4[2]) + (p1[0] + rbf(bf2f((unsigned short)n4[2]) * bf2f((unsigned short)dk4[2]))) * p1[1];
      res[3] = bf2f((unsigned short)x4[3]) + (p1[2] + rbf(bf2f((unsigned short)n4[3]) * bf2f((unsigned short)dk4[3]))) * p1[3];
      *(f32x4*)(out + o) = res;
    }
  }
}

extern "C" void kernel_launch(void* const* d_in, const int* in_sizes, int n_in,
                              void* d_out, int out_size, void* d_ws, size_t ws_size,
                              hipStream_t stream) {
  const bf16* x = (const bf16*)d_in[0];
  const bf16* lam_re = (const bf16*)d_in[1];
  const bf16* lam_im = (const bf16*)d_in[2];
  const bf16* b_re = (const bf16*)d_in[3];
  const bf16* b_im = (const bf16*)d_in[4];
  const bf16* c_re = (const bf16*)d_in[5];
  const bf16* c_im = (const bf16*)d_in[6];
  const bf16* log_dt = (const bf16*)d_in[7];
  const bf16* dskip = (const bf16*)d_in[8];
  const bf16* wg = (const bf16*)d_in[10];
  const bf16* wu = (const bf16*)d_in[11];
  const bf16* wd = (const bf16*)d_in[12];
  char* ws = (char*)d_ws;
  float* out = (float*)d_out;

  hipFuncSetAttribute((const void*)gemm1_kernel, hipFuncAttributeMaxDynamicSharedMemorySize, G1_NST * G1_STAGE);
  hipFuncSetAttribute((const void*)gemm2_kernel, hipFuncAttributeMaxDynamicSharedMemorySize, G2_NST * G2_STAGE);

  prep_kernel<<<PREP_BLOCKS, 256, 0, stream>>>(x, lam_re, lam_im, b_re, b_im, c_re, c_im, log_dt, ws, wg, wu);
  gemm1_kernel<<<256, 512, G1_NST * G1_STAGE, stream>>>(wg, wu, ws);
  scanC_kernel<<<4 * NCHUNK + 448, 256, 0, stream>>>(ws, wd);
  gemm2_kernel<<<256, 256, G2_NST * G2_STAGE, stream>>>(x, wd, dskip, ws, out);
}
```

```cpp
#include <hip/hip_runtime.h>
#include <hip/hip_bf16.h>
#include <stdint.h>

typedef __hip_bfloat16 bf16;
typedef __attribute__((ext_vector_type(8))) short bf16x8;
typedef __attribute__((ext_vector_type(4))) short bf16x4;
typedef __attribute__((ext_vector_type(4))) float f32x4;
typedef __attribute__((__vector_size__(4 * sizeof(short)))) short v4s;

constexpr int L_SEQ = 2048, DM = 1024, FF = 3584, PP = 1024;
constexpr int LDW = 3584;
constexpr int LDXN = DM + 64;
constexpr int KAH = FF + 2 * PP + 64;
constexpr int SCAN_T = 32, NCHUNK = L_SEQ / SCAN_T;

constexpr size_t OFF_XN   = 0;
constexpr size_t OFF_BT   = OFF_XN + (size_t)L_SEQ * LDXN * 2;
constexpr size_t OFF_CT   = OFF_BT + (size_t)DM * LDW * 2;
constexpr size_t OFF_AH   = OFF_CT + (size_t)2 * PP * DM * 2;
constexpr size_t OFF_BU   = OFF_AH + (size_t)L_SEQ * KAH * 2;
constexpr size_t OFF_E    = OFF_BU + (size_t)L_SEQ * PP * 8;
constexpr size_t OFF_LAM  = OFF_E + (size_t)NCHUNK * PP * 8;
constexpr size_t OFF_COEF = OFF_LAM + (size_t)PP * 8;

__device__ __forceinline__ float bf2f(unsigned short u) { return __uint_as_float(((unsigned)u) << 16); }
__device__ __forceinline__ unsigned short f2bf(float f) {
  __bf16 b = (__bf16)f;
  return __builtin_bit_cast(unsigned short, b);
}
__device__ __forceinline__ float rbf(float f) { return bf2f(f2bf(f)); }

constexpr int PREP_LN_BLOCKS = L_SEQ / 4;
constexpr int PREP_TR_BLOCKS = 4 * (1024 / 64) * (1024 / 64);
constexpr int PREP_PF_BLOCKS = 448;
constexpr int PREP_BLOCKS = PREP_LN_BLOCKS + PREP_TR_BLOCKS + 4 + PREP_PF_BLOCKS;

__global__ __launch_bounds__(256) void prep_kernel(
    const bf16* __restrict__ x, const bf16* __restrict__ lam_re, const bf16* __restrict__ lam_im,
    const bf16* __restrict__ b_re, const bf16* __restrict__ b_im,
    const bf16* __restrict__ c_re, const bf16* __restrict__ c_im,
    const bf16* __restrict__ log_dt, char* __restrict__ ws,
    const bf16* __restrict__ wg, const bf16* __restrict__ wu) {
  __shared__ unsigned short tile[64][66];
  const int tid = threadIdx.x;
  if (blockIdx.x < PREP_PF_BLOCKS) {
    const int m = blockIdx.x / 224, part = blockIdx.x % 224;
    const bf16x8* src = (const bf16x8*)(m == 0 ? wg : wu) + (size_t)part * 2048 + tid;
    bf16x8 v[8];
#pragma unroll
    for (int i = 0; i < 8; ++i) v[i] = src[i * 256];
    asm volatile("" :: "v"(v[0]), "v"(v[1]), "v"(v[2]), "v"(v[3]), "v"(v[4]), "v"(v[5]), "v"(v[6]), "v"(v[7]));
    return;
  }
  const int bid = blockIdx.x - PREP_PF_BLOCKS;
  if (bid < PREP_LN_BLOCKS) {
    const int wave = tid >> 6, lane = tid & 63;
    const int row = bid * 4 + wave;
    const unsigned short* xr = (const unsigned short*)x + (size_t)row * DM;
    float v[16];
    bf16x8 a = *(const bf16x8*)(xr + lane * 8);
    bf16x8 b = *(const bf16x8*)(xr + 512 + lane * 8);
    float sum = 0.f;
#pragma unroll
    for (int j = 0; j < 8; ++j) { v[j] = bf2f((unsigned short)a[j]); v[8 + j] = bf2f((unsigned short)b[j]); }
#pragma unroll
    for (int j = 0; j < 16; ++j) sum += v[j];
#pragma unroll
    for (int o = 32; o > 0; o >>= 1) sum += __shfl_xor(sum, o);
    const float mean = sum * (1.0f / DM);
    float sq = 0.f;
#pragma unroll
    for (int j = 0; j < 16; ++j) { float d = v[j] - mean; sq += d * d; }
#pragma unroll
    for (int o = 32; o > 0; o >>= 1) sq += __shfl_xor(sq, o);
    const float var_b = rbf(sq * (1.0f / DM));
    const float mean_b = rbf(mean);
    const float den = rbf(sqrtf(rbf(var_b + 1e-5f)));
    bf16x8 oa, ob;
#pragma unroll
    for (int j = 0; j < 8; ++j) {
      oa[j] = (short)f2bf(rbf(v[j] - mean_b) / den);
      ob[j] = (short)f2bf(rbf(v[8 + j] - mean_b) / den);
    }
    unsigned short* xo = (unsigned short*)(ws + OFF_XN) + (size_t)row * LDXN;
    *(bf16x8*)(xo + lane * 8) = oa;
    *(bf16x8*)(xo + 512 + lane * 8) = ob;
  } else if (bid < PREP_LN_BLOCKS + PREP_TR_BLOCKS) {
    const int t = bid - PREP_LN_BLOCKS;
    const int mat = t >> 8, ti = (t >> 4) & 15, tj = t & 15;
    const unsigned short* src = (const unsigned short*)(mat == 0 ? b_re : mat == 1 ? b_im : mat == 2 ? c_re : c_im);
    const int tc = tid & 7, tr = tid >> 3;
#pragma unroll
    for (int i = 0; i < 2; ++i) {
      const int r = tr + 32 * i;
      bf16x8 v = *(const bf16x8*)(src + (size_t)(ti * 64 + r) * 1024 + tj * 64 + tc * 8);
#pragma unroll
      for (int e = 0; e < 8; ++e) tile[r][tc * 8 + e] = (unsigned short)v[e];
    }
    __syncthreads();
    unsigned short* dst; size_t ld; unsigned short neg = 0;
    if (mat < 2) { dst = (unsigned short*)(ws + OFF_BT) + mat * 1024; ld = LDW; }
    else { dst = (unsigned short*)(ws + OFF_CT) + (size_t)(mat - 2) * 1024 * DM; ld = DM; neg = (mat == 3) ? 0x8000 : 0; }
#pragma unroll
    for (int i = 0; i < 2; ++i) {
      const int c = tr + 32 * i;
      bf16x8 v;
#pragma unroll
      for (int e = 0; e < 8; ++e) v[e] = (short)(tile[tc * 8 + e][c] ^ neg);
      *(bf16x8*)(dst + (size_t)(tj * 64 + c) * ld + ti * 64 + tc * 8) = v;
    }
  } else {
    const int p = (bid - PREP_LN_BLOCKS - PREP_TR_BLOCKS) * 256 + tid;
    const float lr = bf2f(((const unsigned short*)lam_re)[p]);
    const float li = bf2f(((const unsigned short*)lam_im)[p]);
    const float dt = rbf(expf(bf2f(((const unsigned short*)log_dt)[p])));
    const float ar = dt * lr, ai = dt * li;
    const float e = expf(ar);
    float sn, cs; sincosf(ai, &sn, &cs);
    const float br_ = e * cs, bi_ = e * sn;
    const float nr = br_ - 1.0f, ni = bi_;
    const float den = lr * lr + li * li;
    const float cr = (nr * lr + ni * li) / den, ci = (ni * lr - nr * li) / den;
    ((float2*)(ws + OFF_LAM))[p] = make_float2(br_, bi_);
    ((float2*)(ws + OFF_COEF))[p] = make_float2(cr, ci);
  }
}

__device__ __forceinline__ int a_lds_byte(int r, int c) {
  int st = (r >> 4) * 2 + (c >> 5), rr = r & 15, cc = c & 31, ob = rr * 64 + cc * 2;
  return st * 1024 + (ob ^ (((ob >> 9) & 1) << 5));
}
__device__ __forceinline__ void a_stage_rc(int b, int& R, int& C) {
  int st = b >> 10, sb = b & 1023, swz = sb ^ (((sb >> 9) & 1) << 5);
  R = (st >> 1) * 16 + (swz >> 6); C = (st & 1) * 32 + ((swz & 63) >> 1);
}
__device__ __forceinline__ bf16x8 tr_frag(const char* lds_lo, const char* lds_hi) {
  v4s lo = __builtin_amdgcn_ds_read_tr16_b64_v4i16((__attribute__((address_space(3))) v4s*)(lds_lo));
  v4s hi = __builtin_amdgcn_ds_read_tr16_b64_v4i16((__attribute__((address_space(3))) v4s*)(lds_hi));
  bf16x8 r;
  r[0] = lo[0]; r[1] = lo[1]; r[2] = lo[2]; r[3] = lo[3];
  r[4] = hi[0]; r[5] = hi[1]; r[6] = hi[2]; r[7] = hi[3];
  return r;
}
typedef __attribute__((ext_vector_type(2))) unsigned u32x2;
typedef __attribute__((ext_vector_type(4))) unsigned u32x4;
template <int OFF> __device__ __forceinline__ u32x2 lds_tr64(unsigned addr) {
  u32x2 r; asm volatile("ds_read_b64_tr_b16 %0, %1 offset:%2" : "=v"(r) : "v"(addr), "i"(OFF) : "memory"); return r;
}
template <int OFF> __device__ __forceinline__ bf16x8 lds_b128(unsigned addr) {
  u32x4 r; asm volatile("ds_read_b128 %0, %1 offset:%2" : "=v"(r) : "v"(addr), "i"(OFF) : "memory"); return __builtin_bit_cast(bf16x8, r);
}
__device__ __forceinline__ bf16x8 join_frag(u32x2 lo, u32x2 hi) {
  u32x4 r = __builtin_shufflevector(lo, hi, 0, 1, 2, 3); return __builtin_bit_cast(bf16x8, r);
}
#define WAIT_LGKM(n) do { asm volatile("s_waitcnt lgkmcnt(" #n ")" ::: "memory"); __builtin_amdgcn_sched_barrier(0); } while (0)
#define GLDS16(gptr, ldsptr) __builtin_amdgcn_global_load_lds((const void __attribute__((address_space(1)))*)(gptr), (void __attribute__((address_space(3)))*)(ldsptr), 16, 0, 0)

__device__ __forceinline__ float xor8(float v) {
  return __int_as_float(__builtin_amdgcn_mov_dpp(__float_as_int(v), 0x128, 0xf, 0xf, false));
}
template <int MASK> __device__ __forceinline__ float sel_xor8(float old, float src) {
  return __int_as_float(__builtin_amdgcn_update_dpp(__float_as_int(old), __float_as_int(src), 0x128, 0xf, MASK, false));
}
__device__ __forceinline__ float gelu_tanh_f(float x) {
  const float c1 = -2.0f * 0.7978845608028654f * 1.4426950408889634f, c3 = c1 * 0.044715f;
  const float e = __builtin_amdgcn_exp2f(x * __builtin_fmaf(c3, x * x, c1));
  return x * __builtin_amdgcn_rcpf(1.0f + e);
}

constexpr int G1_BM = 256, G1_BN = 288, G1_CH = G1_BN / 8;
constexpr int G1_BK = 32, G1_NST = 4, G1_NK = 1024 / G1_BK;
constexpr int G1_A_BYTES = G1_BM * G1_BK * 2, G1_B_BYTES = G1_BK * G1_BN * 2, G1_STAGE = G1_A_BYTES + G1_B_BYTES;
__device__ __forceinline__ int g1_swz(int k) { return ((k >> 3) & 1) << 1; }
#define WAIT_VM(n) asm volatile("s_waitcnt vmcnt(" #n ") lgkmcnt(0)" ::: "memory")
#define RAW_BAR() __builtin_amdgcn_s_barrier()

__global__ __launch_bounds__(512, 2) void gemm1_kernel(
    const bf16* __restrict__ wg, const bf16* __restrict__ wu, char* __restrict__ ws) {
  extern __shared__ __attribute__((aligned(16))) char smem[];
  const int tid = threadIdx.x, lane = tid & 63, wid = __builtin_amdgcn_readfirstlane(tid >> 6);
  const int bid = blockIdx.x;
  const int xcd = bid & 7, idx = bid >> 3;
  const int pn = xcd * 4 + (idx & 3), pm = idx >> 2;
  const int brow = pm * G1_BM;
  const int wr = wid & 3, wc = wid >> 2;
  const unsigned short* xn = (const unsigned short*)(ws + OFF_XN);
  const unsigned short* bt = (const unsigned short*)(ws + OFF_BT);

  unsigned aoffs[2];
#pragma unroll
  for (int i = 0; i < 2; ++i) {
    int sb = lane * 16, swz = sb ^ (((sb >> 9) & 1) << 5);
    int R = (i * 8 + wid) * 16 + (swz >> 6), C = (swz & 63) >> 1;
    aoffs[i] = (unsigned)((brow + R) * LDXN + C);
  }
  const unsigned short* bsrc[3];
#pragma unroll
  for (int i = 0; i < 3; ++i) {
    int inst = i * 8 + wid; if (inst > 17) inst = 17;
    int Lc = inst * 64 + lane, k = Lc / G1_CH, cp = Lc % G1_CH, c = cp ^ g1_swz(k);
    int half = c & 1, blk = c >> 1, w_ = blk >= 9, jj = blk - w_ * 9;
    const unsigned short* base = (const unsigned short*)(half ? wu : wg);
    int o = pn * 112 + w_ * 56 + jj * 8;
    if (jj >= 7) { base = bt; o = half * 1024 + pn * 32 + w_ * 16 + (jj - 7) * 8; }
    bsrc[i] = base + (k * LDW + o);
  }
#define G1_STAGE_LOADS(buf) do { \
    char* sa_ = smem + (buf) * G1_STAGE; char* sb_ = sa_ + G1_A_BYTES; \
    _Pragma("unroll") for (int i = 0; i < 2; ++i) { GLDS16(xn + aoffs[i], sa_ + (i * 8 + wid) * 1024); aoffs[i] += G1_BK; } \
    _Pragma("unroll") for (int i = 0; i < 2; ++i) { GLDS16(bsrc[i], sb_ + (i * 8 + wid) * 1024); bsrc[i] += G1_BK * LDW; } \
    if (wid < 2) { GLDS16(bsrc[2], sb_ + (16 + wid) * 1024); bsrc[2] += G1_BK * LDW; } \
  } while (0)
  f32x4 acc[4][9];
#pragma unroll
  for (int i = 0; i < 4; ++i)
#pragma unroll
    for (int j = 0; j < 9; ++j) acc[i][j] = (f32x4){0.f, 0.f, 0.f, 0.f};

  const int fr = lane & 15, fq = lane >> 4, tq = (lane & 15) >> 2, tp = lane & 3;
  const int ob_ = fr * 64 + fq * 16, aoff0 = (wr * 4) * 1024 + (ob_ ^ (((ob_ >> 9) & 1) << 5));
  const int bRow = G1_A_BYTES + ((8 * fq + tq) * G1_CH + (tp >> 1)) * 16 + 8 * (tp & 1) + 32 * 9 * wc;
  const int sgn = 32 * (fq & 1);
  const unsigned ldsb = (unsigned)(size_t)(__attribute__((address_space(3))) char*)smem;
  const unsigned aA = ldsb + aoff0;
  const unsigned aE = ldsb + bRow + (wc ? -sgn : sgn);
  const unsigned aO = ldsb + bRow + (wc ? sgn : -sgn);
#define G1_RDB1(jj, ad) join_frag(lds_tr64<32 * (jj)>((ad) + bo_), lds_tr64<32 * (jj) + 4 * G1_CH * 16>((ad) + bo_))
#define G1_READ(buf) do { \
    const unsigned bo_ = (buf) * G1_STAGE; \
    bfr[0] = G1_RDB1(0, aE); bfr[1] = G1_RDB1(1, aO); bfr[2] = G1_RDB1(2, aE); \
    af[0] = lds_b128<0>(aA + bo_); af[1] = lds_b128<1024>(aA + bo_); af[2] = lds_b128<2048>(aA + bo_); af[3] = lds_b128<3072>(aA + bo_); \
    bfr[3] = G1_RDB1(3, aO); bfr[4] = G1_RDB1(4, aE); bfr[5] = G1_RDB1(5, aO); \
    bfr[6] = G1_RDB1(6, aE); bfr[7] = G1_RDB1(7, aO); bfr[8] = G1_RDB1(8, aE); \
  } while (0)
#define G1_MMA() do { \
    __builtin_amdgcn_s_setprio(1); \
    _Pragma("unroll") for (int jj = 0; jj < 9; ++jj) \
      _Pragma("unroll") for (int mi = 0; mi < 4; ++mi) \
        acc[mi][jj] = __builtin_amdgcn_mfma_f32_16x16x32_bf16(af[mi], bfr[jj], acc[mi][jj], 0, 0, 0); \
    __builtin_amdgcn_s_setprio(0); \
  } while (0)
#define G1_WAITN(n2, n1) do { if (wid < 2) WAIT_VM(n2); else WAIT_VM(n1); __builtin_amdgcn_sched_barrier(0); } while (0)
#define G1_STEP(u, LOADS, w2, w1) do { \
    G1_READ(u); \
    LOADS; \
    G1_WAITN(w2, w1); RAW_BAR(); \
    G1_MMA(); \
    G1_WAITN(w2, w1); RAW_BAR(); \
  } while (0)

  bf16x8 af[4], bfr[9];
  G1_STAGE_LOADS(0); G1_STAGE_LOADS(1); G1_STAGE_LOADS(2);
  G1_WAITN(10, 8); RAW_BAR();
  if (wc == 1) RAW_BAR();
  for (int kt0 = 0; kt0 < G1_NK - 4; kt0 += 4) {
    G1_STEP(0, G1_STAGE_LOADS(3), 10, 8);
    G1_STEP(1, G1_STAGE_LOADS(0), 10, 8);
    G1_STEP(2, G1_STAGE_LOADS(1), 10, 8);
    G1_STEP(3, G1_STAGE_LOADS(2), 10, 8);
  }
  G1_STEP(0, G1_STAGE_LOADS(3), 10, 8);
  G1_STEP(1, , 5, 4);
  G1_STEP(2, , 0, 0);
  G1_STEP(3, , 0, 0);
  if (wc == 0) RAW_BAR();

  int tz = threadIdx.x; asm volatile("" : "+v"(tz));
  const int lane2 = tz & 63, fr2 = tz & 15, fq2 = (tz >> 4) & 3;
  const bool hi = fr2 >= 8;
  const int f8 = fr2 & 7, rb = fq2 * 4 + (hi ? 2 : 0);
  unsigned short* ah = (unsigned short*)(ws + OFF_AH);
  float2* bu = (float2*)(ws + OFF_BU);
  const float2* coef = (const float2*)(ws + OFF_COEF);
  const float2* lamb = (const float2*)(ws + OFF_LAM);
  float2* Eo = (float2*)(ws + OFF_E);
  const int P0 = pn * 112 + wc * 56;
  const int S0 = pn * 32 + wc * 16;
  constexpr int nf = 7;
  RAW_BAR();
  constexpr int EP_LD = 160;
  char* wl = smem + wid * (64 * EP_LD);
#pragma unroll
  for (int jj = 0; jj < 9; ++jj) {
    if (jj < nf) {
#pragma unroll
      for (int mi = 0; mi < 4; ++mi) {
        const f32x4 v = acc[mi][jj];
        const float g0 = rbf(sel_xor8<0xC>(v[0], v[2])), g1 = rbf(sel_xor8<0xC>(v[1], v[3]));
        const float u0 = rbf(sel_xor8<0x3>(v[2], v[0])), u1 = rbf(sel_xor8<0x3>(v[3], v[1]));
        char* d = wl + (mi * 16 + rb) * EP_LD + (jj * 8 + f8) * 2;
        *(unsigned short*)(d) = f2bf(rbf(gelu_tanh_f(g0)) * u0);
        *(unsigned short*)(d + EP_LD) = f2bf(rbf(gelu_tanh_f(g1)) * u1);
      }
    } else {
      const int p = S0 + (jj - 7) * 8 + f8;
      const float2 cf = coef[p];
      const float2 lam = lamb[p];
      const float l2r = lam.x * lam.x - lam.y * lam.y, l2i = 2.f * lam.x * lam.y;
      const float l4r = l2r * l2r - l2i * l2i, l4i = 2.f * l2r * l2i;
      const float l8r = l4r * l4r - l4i * l4i, l8i = 2.f * l4r * l4i;
      const float l16r = l8r * l8r - l8i * l8i, l16i = 2.f * l8r * l8i;
      float mr = (fq2 & 1) ? 1.f : l4r, mim = (fq2 & 1) ? 0.f : l4i;
      if (!(fq2 & 2)) { const float a_ = mr * l8r - mim * l8i, b_ = mr * l8i + mim * l8r; mr = a_; mim = b_; }
      if (!hi) { const float a_ = mr * l2r - mim * l2i, b_ = mr * l2i + mim * l2r; mr = a_; mim = b_; }
      float er[4], ei[4];
#pragma unroll
      for (int mi = 0; mi < 4; ++mi) {
        const f32x4 v = acc[mi][jj];
        const float ur0 = sel_xor8<0xC>(v[0], v[2]), ur1 = sel_xor8<0xC>(v[1], v[3]);
        const float ui0 = sel_xor8<0x3>(v[2], v[0]), ui1 = sel_xor8<0x3>(v[3], v[1]);
        const float b0r = cf.x * ur0 - cf.y * ui0, b0i = cf.x * ui0 + cf.y * ur0;
        const float b1r = cf.x * ur1 - cf.y * ui1, b1i = cf.x * ui1 + cf.y * ur1;
        const int row = brow + wr * 64 + mi * 16 + rb;
        bu[(size_t)row * PP + p] = make_float2(b0r, b0i);
        bu[(size_t)(row + 1) * PP + p] = make_float2(b1r, b1i);
        er[mi] = lam.x * b0r - lam.y * b0i + b1r; ei[mi] = lam.x * b0i + lam.y * b0r + b1i;
      }
#pragma unroll
      for (int ch = 0; ch < 2; ++ch) {
        const float tr_ = l16r * er[2 * ch] - l16i * ei[2 * ch] + er[2 * ch + 1], ti_ = l16r * ei[2 * ch] + l16i * er[2 * ch] + ei[2 * ch + 1];
        float sr = mr * tr_ - mim * ti_, si = mr * ti_ + mim * tr_;
        sr += xor8(sr); si += xor8(si);
        sr += __shfl_xor(sr, 16); si += __shfl_xor(si, 16);
        sr += __shfl_xor(sr, 32); si += __shfl_xor(si, 32);
        if (lane2 < 8) Eo[(size_t)(((brow + wr * 64) >> 5) + ch) * PP + p] = make_float2(sr, si);
      }
    }
  }
  asm volatile("s_waitcnt lgkmcnt(0)" ::: "memory");
  {
    unsigned short* dst0 = ah + (size_t)(brow + wr * 64) * KAH + P0;
#pragma unroll
    for (int i = 0; i < 7; ++i) {
      const int q = i * 64 + lane2, row = q / 7, ch = q % 7;
      {
        bf16x8 v = *(const bf16x8*)(wl + row * EP_LD + ch * 16);
        *(bf16x8*)(dst0 + (size_t)row * KAH + ch * 8) = v;
      }
    }
  }
}

__global__ __launch_bounds__(256) void scanA_kernel(char* __restrict__ ws) {
  const int p = (blockIdx.x & 3) * 256 + threadIdx.x, c = blockIdx.x >> 2;
  const float2 lam = ((const float2*)(ws + OFF_LAM))[p];
  const float2* bu = (const float2*)(ws + OFF_BU) + (size_t)c * SCAN_T * PP + p;
  float er = 0.f, ei = 0.f;
#pragma unroll 8
  for (int s = 0; s < SCAN_T; ++s) {
    float2 b = bu[(size_t)s * PP];
    float nr = lam.x * er - lam.y * ei + b.x, ni = lam.x * ei + lam.y * er + b.y;
    er = nr; ei = ni;
  }
  ((float2*)(ws + OFF_E))[(size_t)c * PP + p] = make_float2(er, ei);
}

__global__ __launch_bounds__(256) void scanC_kernel(char* __restrict__ ws, const bf16* __restrict__ wd) {
  if (blockIdx.x >= 4 * NCHUNK) {
    const bf16x8* src = (const bf16x8*)wd + (size_t)(blockIdx.x - 4 * NCHUNK) * 1024 + threadIdx.x;
    bf16x8 v0 = src[0], v1 = src[256], v2 = src[512], v3 = src[768];
    asm volatile("" :: "v"(v0), "v"(v1), "v"(v2), "v"(v3));
    return;
  }
  const int p = (blockIdx.x & 3) * 256 + threadIdx.x, c = blockIdx.x >> 2;
  const float2* bu = (const float2*)(ws + OFF_BU) + (size_t)c * SCAN_T * PP + p;
  float2 b[SCAN_T];
#pragma unroll
  for (int s = 0; s < SCAN_T; ++s) b[s] = bu[(size_t)s * PP];
  const float2 lam = ((const float2*)(ws + OFF_LAM))[p];
  const float2* E = (const float2*)(ws + OFF_E) + p;
  float lr = lam.x, li = lam.y;
#pragma unroll
  for (int i = 0; i < 5; ++i) { float a = lr * lr - li * li, bb = 2.f * lr * li; lr = a; li = bb; }
  float hr = 0.f, hi = 0.f;
  {
    float2 e[NCHUNK - 1];
#pragma unroll
    for (int u = 0; u < NCHUNK - 1; ++u) e[u] = (u < c) ? E[(size_t)u * PP] : make_float2(0.f, 0.f);
#pragma unroll
    for (int u = 0; u < NCHUNK - 1; ++u) if (u < c) { float nr = lr * hr - li * hi + e[u].x, ni = lr * hi + li * hr + e[u].y; hr = nr; hi = ni; }
  }
  unsigned short* ah = (unsigned short*)(ws + OFF_AH) + (size_t)c * SCAN_T * KAH + FF + p;
#pragma unroll
  for (int s = 0; s < SCAN_T; ++s) {
    float nr = lam.x * hr - lam.y * hi + b[s].x, ni = lam.x * hi + lam.y * hr + b[s].y;
    hr = nr; hi = ni;
    ah[(size_t)s * KAH] = f2bf(hr);
    ah[(size_t)s * KAH + PP] = f2bf(hi);
  }
}

constexpr int G2_BM = 128, G2_BN = 64, G2_CH = G2_BN / 8;
constexpr int G2_A_BYTES = G2_BM * 64 * 2, G2_B_BYTES = 64 * G2_BN * 2, G2_STAGE = G2_A_BYTES + G2_B_BYTES;
constexpr int G2_NST = 5;
__device__ __forceinline__ int g2_swz(int k) { return (((k >> 1) & 1) << 1) | (((k >> 3) & 1) << 2); }

__global__ __launch_bounds__(256) void gemm2_kernel(
    const bf16* __restrict__ x, const bf16* __restrict__ wd, const bf16* __restrict__ dskip,
    const char* __restrict__ ws, float* __restrict__ out) {
  extern __shared__ __attribute__((aligned(16))) char smem[];
  const int tid = threadIdx.x, lane = tid & 63, wid = __builtin_amdgcn_readfirstlane(tid >> 6);
  const int bid = blockIdx.x;
  const int xcd = bid & 7, idx = bid >> 3;
  const int pm = (xcd & 3) * 4 + (idx & 3), pn = (xcd >> 2) * 8 + (idx >> 2);
  const int brow = pm * G2_BM, bcol = pn * G2_BN;
  const unsigned short* ah = (const unsigned short*)(ws + OFF_AH);
  const unsigned short* ct = (const unsigned short*)(ws + OFF_CT);
  const unsigned short* asrc[4];
#pragma unroll
  for (int i = 0; i < 4; ++i) {
    int R = (i * 4 + wid) * 8 + (lane >> 3), C = ((lane & 7) ^ (R & 7)) * 8;
    asrc[i] = ah + (size_t)(brow + R) * KAH + C;
  }
  int boffs[2];
#pragma unroll
  for (int i = 0; i < 2; ++i) {
    int Lc = (i * 4 + wid) * 64 + lane, k = Lc / G2_CH, cp = Lc % G2_CH, c = cp ^ g2_swz(k);
    boffs[i] = k * DM + bcol + c * 8;
  }
#define ABL_A(x) x
#define ABL_B(x) x
#define G2_STAGE_LOADS(buf, kt) do { \
    char* sa_ = smem + (buf) * G2_STAGE; char* sb_ = sa_ + G2_A_BYTES; \
    ABL_A(_Pragma("unroll") for (int i = 0; i < 4; ++i) GLDS16(asrc[i] + (kt) * 64, sa_ + (i * 4 + wid) * 1024);) \
    const unsigned short* bb_ = (kt) < 56 ? (const unsigned short*)wd + (size_t)(kt) * 64 * DM : ct + (size_t)((kt) - 56) * 64 * DM; \
    ABL_B(_Pragma("unroll") for (int i = 0; i < 2; ++i) GLDS16(bb_ + boffs[i], sb_ + (i * 4 + wid) * 1024);) \
  } while (0)
  const int wr = wid >> 1, wc = wid & 1;
  const int fr = lane & 15, fq = lane >> 4, tq = (lane & 15) >> 2, tp = lane & 3;
  f32x4 accd[4][2], accy[4][2];
#pragma unroll
  for (int i = 0; i < 4; ++i)
#pragma unroll
    for (int j = 0; j < 2; ++j) { accd[i][j] = (f32x4){0.f, 0.f, 0.f, 0.f}; accy[i][j] = (f32x4){0.f, 0.f, 0.f, 0.f}; }

  const unsigned ldsb = (unsigned)(size_t)(__attribute__((address_space(3))) char*)smem;
  const unsigned aA0 = ldsb + (wr * 64 + fr) * 128 + ((fq ^ (fr & 7)) * 16);
  const unsigned aA1 = ldsb + (wr * 64 + fr) * 128 + (((4 + fq) ^ (fr & 7)) * 16);
  unsigned aB[2][2];
#pragma unroll
  for (int nj = 0; nj < 2; ++nj)
#pragma unroll
    for (int t = 0; t < 2; ++t) {
      const int k0 = 8 * fq + tq + 4 * t, j = wc * 2 + nj;
      aB[nj][t] = ldsb + G2_A_BYTES + (k0 * G2_CH + ((2 * j + (tp >> 1)) ^ g2_swz(k0))) * 16 + 8 * (tp & 1);
    }
#define G2_RD(af, bfm, buf, kk) do { \
    bfm[0] = join_frag(lds_tr64<(kk) * 32 * G2_CH * 16>(aB[0][0] + (buf) * G2_STAGE), lds_tr64<(kk) * 32 * G2_CH * 16>(aB[0][1] + (buf) * G2_STAGE)); \
    bfm[1] = join_frag(lds_tr64<(kk) * 32 * G2_CH * 16>(aB[1][0] + (buf) * G2_STAGE), lds_tr64<(kk) * 32 * G2_CH * 16>(aB[1][1] + (buf) * G2_STAGE)); \
    af[0] = lds_b128<0>(((kk) ? aA1 : aA0) + (buf) * G2_STAGE); af[1] = lds_b128<2048>(((kk) ? aA1 : aA0) + (buf) * G2_STAGE); \
    af[2] = lds_b128<4096>(((kk) ? aA1 : aA0) + (buf) * G2_STAGE); af[3] = lds_b128<6144>(((kk) ? aA1 : aA0) + (buf) * G2_STAGE); \
  } while (0)
#define G2_MMA(acc, af, bfm) do { \
    _Pragma("unroll") for (int mi = 0; mi < 4; ++mi) \
      _Pragma("unroll") for (int nj = 0; nj < 2; ++nj) acc[mi][nj] = __builtin_amdgcn_mfma_f32_16x16x32_bf16(af[mi], bfm[nj], acc[mi][nj], 0, 0, 0); \
  } while (0)
#define compute(buf, acc) do { \
    bf16x8 a0[4], a1[4], f0[2], f1[2]; \
    G2_RD(a0, f0, buf, 0); \
    WAIT_LGKM(0); \
    G2_RD(a1, f1, buf, 1); \
    G2_MMA(acc, a0, f0); \
    WAIT_LGKM(0); \
    G2_MMA(acc, a1, f1); \
  } while (0)
  G2_STAGE_LOADS(0, 0); G2_STAGE_LOADS(1, 1); G2_STAGE_LOADS(2, 2); G2_STAGE_LOADS(3, 3);
#define G2_STEP(u, kt, acc) do { WAIT_VM(18); RAW_BAR(); G2_STAGE_LOADS(((u) + 4) % 5, (kt) + 4); compute(u, acc); } while (0)
  for (int kt0 = 0; kt0 < 55; kt0 += 5) {
    G2_STEP(0, kt0, accd); G2_STEP(1, kt0 + 1, accd); G2_STEP(2, kt0 + 2, accd); G2_STEP(3, kt0 + 3, accd); G2_STEP(4, kt0 + 4, accd);
  }
  WAIT_VM(18); RAW_BAR(); G2_STAGE_LOADS(4, 59); compute(0, accd);
  for (int kt0 = 56; kt0 < 81; kt0 += 5) {
    G2_STEP(1, kt0, accy); G2_STEP(2, kt0 + 1, accy); G2_STEP(3, kt0 + 2, accy); G2_STEP(4, kt0 + 3, accy); G2_STEP(0, kt0 + 4, accy);
  }
  WAIT_VM(18); RAW_BAR(); G2_STAGE_LOADS(0, 85); compute(1, accy);
  WAIT_VM(18); RAW_BAR(); G2_STAGE_LOADS(1, 86); compute(2, accy);
  WAIT_VM(18); RAW_BAR(); G2_STAGE_LOADS(2, 87); compute(3, accy);
  WAIT_VM(18); RAW_BAR(); compute(4, accy);
  WAIT_VM(12); RAW_BAR(); compute(0, accy);
  WAIT_VM(6); RAW_BAR(); compute(1, accy);
  WAIT_VM(0); RAW_BAR(); compute(2, accy);
  const unsigned short* xs = (const unsigned short*)x;
  const unsigned short* xn = (const unsigned short*)(ws + OFF_XN);
  WAIT_VM(0); RAW_BAR();
  constexpr int EP_LD = 32 * 8 + 16;
  char* wl = smem + wid * (64 * EP_LD);
#pragma unroll
  for (int nj = 0; nj < 2; ++nj)
#pragma unroll
    for (int mi = 0; mi < 4; ++mi)
#pragma unroll
      for (int r = 0; r < 4; ++r) {
        const float dn = rbf(accd[mi][nj][r]);
        const float g = rbf(dn * __builtin_amdgcn_rcpf(1.0f + __expf(-dn)));
        *(float2*)(wl + (mi * 16 + fq * 4 + r) * EP_LD + (nj * 16 + fr) * 8) = make_float2(2.0f * accy[mi][nj][r], g);
      }
  asm volatile("s_waitcnt lgkmcnt(0)" ::: "memory");
  {
    const int c4 = (lane & 7) * 4, rsub = lane >> 3;
    const int col = bcol + wc * 32 + c4;
    const unsigned short* dk = (const unsigned short*)dskip + col;
    const bf16x4 dk4 = *(const bf16x4*)dk;
#pragma unroll
    for (int i = 0; i < 8; ++i) {
      const int rl = i * 8 + rsub, row = brow + wr * 64 + rl;
      const size_t o = (size_t)row * DM + col;
      const bf16x4 x4 = *(const bf16x4*)(xs + o), n4 = *(const bf16x4*)(xn + (size_t)row * LDXN + col);
      const f32x4 p0 = *(const f32x4*)(wl + rl * EP_LD + c4 * 8), p1 = *(const f32x4*)(wl + rl * EP_LD + c4 * 8 + 16);
      f32x4 res;
      res[0] = bf2f((unsigned short)x4[0]) + (p0[0] + rbf(bf2f((unsigned short)n4[0]) * bf2f((unsigned short)dk4[0]))) * p0[1];
      res[1] = bf2f((unsigned short)x4[1]) + (p0[2] + rbf(bf2f((unsigned short)n4[1]) * bf2f((unsigned short)dk4[1]))) * p0[3];
      res[2] = bf2f((unsigned short)x4[2]) + (p1[0] + rbf(bf2f((unsigned short)n4[2]) * bf2f((unsigned short)dk4[2]))) * p1[1];
      res[3] = bf2f((unsigned short)x4[3]) + (p1[2] + rbf(bf2f((unsigned short)n4[3]) * bf2f((unsigned short)dk4[3]))) * p1[3];
      *(f32x4*)(out + o) = res;
    }
  }
}

extern "C" void kernel_launch(void* const* d_in, const int* in_sizes, int n_in,
                              void* d_out, int out_size, void* d_ws, size_t ws_size,
                              hipStream_t stream) {
  const bf16* x = (const bf16*)d_in[0];
  const bf16* lam_re = (const bf16*)d_in[1];
  const bf16* lam_im = (const bf16*)d_in[2];
  const bf16* b_re = (const bf16*)d_in[3];
  const bf16* b_im = (const bf16*)d_in[4];
  const bf16* c_re = (const bf16*)d_in[5];
  const bf16* c_im = (const bf16*)d_in[6];
  const bf16* log_dt = (const bf16*)d_in[7];
  const bf16* dskip = (const bf16*)d_in[8];
  const bf16* wg = (const bf16*)d_in[10];
  const bf16* wu = (const bf16*)d_in[11];
  const bf16* wd = (const bf16*)d_in[12];
  char* ws = (char*)d_ws;
  float* out = (float*)d_out;

  hipFuncSetAttribute((const void*)gemm1_kernel, hipFuncAttributeMaxDynamicSharedMemorySize, G1_NST * G1_STAGE);
  hipFuncSetAttribute((const void*)gemm2_kernel, hipFuncAttributeMaxDynamicSharedMemorySize, G2_NST * G2_STAGE);

  prep_kernel<<<PREP_BLOCKS, 256, 0, stream>>>(x, lam_re, lam_im, b_re, b_im, c_re, c_im, log_dt, ws, wg, wu);
  gemm1_kernel<<<256, 512, G1_NST * G1_STAGE, stream>>>(wg, wu, ws);
  scanC_kernel<<<4 * NCHUNK + 448, 256, 0, stream>>>(ws, wd);
  gemm2_kernel<<<256, 256, G2_NST * G2_STAGE, stream>>>(x, wd, dskip, ws, out);
}
```

```cpp
#include <hip/hip_runtime.h>
#include <hip/hip_bf16.h>
#include <stdint.h>

typedef __hip_bfloat16 bf16;
typedef __attribute__((ext_vector_type(8))) short bf16x8;
typedef __attribute__((ext_vector_type(4))) short bf16x4;
typedef __attribute__((ext_vector_type(4))) float f32x4;
typedef __attribute__((__vector_size__(4 * sizeof(short)))) short v4s;

constexpr int L_SEQ = 2048, DM = 1024, FF = 3584, PP = 1024;
constexpr int LDW = 3584;
constexpr int LDXN = DM + 64;
constexpr int KAH = FF + 2 * PP + 64;
constexpr int SCAN_T = 32, NCHUNK = L_SEQ / SCAN_T;

constexpr size_t OFF_XN   = 0;
constexpr size_t OFF_BT   = OFF_XN + (size_t)L_SEQ * LDXN * 2;
constexpr size_t OFF_CT   = OFF_BT + (size_t)DM * LDW * 2;
constexpr size_t OFF_AH   = OFF_CT + (size_t)2 * PP * DM * 2;
constexpr size_t OFF_BU   = OFF_AH + (size_t)L_SEQ * KAH * 2;
constexpr size_t OFF_E    = OFF_BU + (size_t)L_SEQ * PP * 8;
constexpr size_t OFF_LAM  = OFF_E + (size_t)NCHUNK * PP * 8;
constexpr size_t OFF_COEF = OFF_LAM + (size_t)PP * 8;

__device__ __forceinline__ float bf2f(unsigned short u) { return __uint_as_float(((unsigned)u) << 16); }
__device__ __forceinline__ unsigned short f2bf(float f) {
  __bf16 b = (__bf16)f;
  return __builtin_bit_cast(unsigned short, b);
}
__device__ __forceinline__ float rbf(float f) { return bf2f(f2bf(f)); }

constexpr int PREP_LN_BLOCKS = L_SEQ / 4;
constexpr int PREP_TR_BLOCKS = 4 * (1024 / 64) * (1024 / 64);
constexpr int PREP_PF_BLOCKS = 448;
constexpr int PREP_BLOCKS = PREP_LN_BLOCKS + PREP_TR_BLOCKS + 4 + PREP_PF_BLOCKS;

__global__ __launch_bounds__(256) void prep_kernel(
    const bf16* __restrict__ x, const bf16* __restrict__ lam_re, const bf16* __restrict__ lam_im,
    const bf16* __restrict__ b_re, const bf16* __restrict__ b_im,
    const bf16* __restrict__ c_re, const bf16* __restrict__ c_im,
    const bf16* __restrict__ log_dt, char* __restrict__ ws,
    const bf16* __restrict__ wg, const bf16* __restrict__ wu) {
  __shared__ unsigned short tile[64][66];
  const int tid = threadIdx.x;
  if (blockIdx.x < PREP_PF_BLOCKS) {
    const int m = blockIdx.x / 224, part = blockIdx.x % 224;
    const bf16x8* src = (const bf16x8*)(m == 0 ? wg : wu) + (size_t)part * 2048 + tid;
    bf16x8 v[8];
#pragma unroll
    for (int i = 0; i < 8; ++i) v[i] = src[i * 256];
    asm volatile("" :: "v"(v[0]), "v"(v[1]), "v"(v[2]), "v"(v[3]), "v"(v[4]), "v"(v[5]), "v"(v[6]), "v"(v[7]));
    return;
  }
  const int bid = blockIdx.x - PREP_PF_BLOCKS;
  if (bid < PREP_LN_BLOCKS) {
    const int wave = tid >> 6, lane = tid & 63;
    const int row = bid * 4 + wave;
    const unsigned short* xr = (const unsigned short*)x + (size_t)row * DM;
    float v[16];
    bf16x8 a = *(const bf16x8*)(xr + lane * 8);
    bf16x8 b = *(const bf16x8*)(xr + 512 + lane * 8);
    float sum = 0.f;
#pragma unroll
    for (int j = 0; j < 8; ++j) { v[j] = bf2f((unsigned short)a[j]); v[8 + j] = bf2f((unsigned short)b[j]); }
#pragma unroll
    for (int j = 0; j < 16; ++j) sum += v[j];
#pragma unroll
    for (int o = 32; o > 0; o >>= 1) sum += __shfl_xor(sum, o);
    const float mean = sum * (1.0f / DM);
    float sq = 0.f;
#pragma unroll
    for (int j = 0; j < 16; ++j) { float d = v[j] - mean; sq += d * d; }
#pragma unroll
    for (int o = 32; o > 0; o >>= 1) sq += __shfl_xor(sq, o);
    const float var_b = rbf(sq * (1.0f / DM));
    const float mean_b = rbf(mean);
    const float den = rbf(sqrtf(rbf(var_b + 1e-5f)));
    bf16x8 oa, ob;
#pragma unroll
    for (int j = 0; j < 8; ++j) {
      oa[j] = (short)f2bf(rbf(v[j] - mean_b) / den);
      ob[j] = (short)f2bf(rbf(v[8 + j] - mean_b) / den);
    }
    unsigned short* xo = (unsigned short*)(ws + OFF_XN) + (size_t)row * LDXN;
    *(bf16x8*)(xo + lane * 8) = oa;
    *(bf16x8*)(xo + 512 + lane * 8) = ob;
  } else if (bid < PREP_LN_BLOCKS + PREP_TR_BLOCKS) {
    const int t = bid - PREP_LN_BLOCKS;
    const int mat = t >> 8, ti = (t >> 4) & 15, tj = t & 15;
    const unsigned short* src = (const unsigned short*)(mat == 0 ? b_re : mat == 1 ? b_im : mat == 2 ? c_re : c_im);
    const int tc = tid & 7, tr = tid >> 3;
#pragma unroll
    for (int i = 0; i < 2; ++i) {
      const int r = tr + 32 * i;
      bf16x8 v = *(const bf16x8*)(src + (size_t)(ti * 64 + r) * 1024 + tj * 64 + tc * 8);
#pragma unroll
      for (int e = 0; e < 8; ++e) tile[r][tc * 8 + e] = (unsigned short)v[e];
    }
    __syncthreads();
    unsigned short* dst; size_t ld; unsigned short neg = 0;
    if (mat < 2) { dst = (unsigned short*)(ws + OFF_BT) + mat * 1024; ld = LDW; }
    else { dst = (unsigned short*)(ws + OFF_CT) + (size_t)(mat - 2) * 1024 * DM; ld = DM; neg = (mat == 3) ? 0x8000 : 0; }
#pragma unroll
    for (int i = 0; i < 2; ++i) {
      const int c = tr + 32 * i;
      bf16x8 v;
#pragma unroll
      for (int e = 0; e < 8; ++e) v[e] = (short)(tile[tc * 8 + e][c] ^ neg);
      *(bf16x8*)(dst + (size_t)(tj * 64 + c) * ld + ti * 64 + tc * 8) = v;
    }
  } else {
    const int p = (bid - PREP_LN_BLOCKS - PREP_TR_BLOCKS) * 256 + tid;
    const float lr = bf2f(((const unsigned short*)lam_re)[p]);
    const float li = bf2f(((const unsigned short*)lam_im)[p]);
    const float dt = rbf(expf(bf2f(((const unsigned short*)log_dt)[p])));
    const float ar = dt * lr, ai = dt * li;
    const float e = expf(ar);
    float sn, cs; sincosf(ai, &sn, &cs);
    const float br_ = e * cs, bi_ = e * sn;
    const float nr = br_ - 1.0f, ni = bi_;
    const float den = lr * lr + li * li;
    const float cr = (nr * lr + ni * li) / den, ci = (ni * lr - nr * li) / den;
    ((float2*)(ws + OFF_LAM))[p] = make_float2(br_, bi_);
    ((float2*)(ws + OFF_COEF))[p] = make_float2(cr, ci);
  }
}

__device__ __forceinline__ int a_lds_byte(int r, int c) {
  int st = (r >> 4) * 2 + (c >> 5), rr = r & 15, cc = c & 31, ob = rr * 64 + cc * 2;
  return st * 1024 + (ob ^ (((ob >> 9) & 1) << 5));
}
__device__ __forceinline__ void a_stage_rc(int b, int& R, int& C) {
  int st = b >> 10, sb = b & 1023, swz = sb ^ (((sb >> 9) & 1) << 5);
  R = (st >> 1) * 16 + (swz >> 6); C = (st & 1) * 32 + ((swz & 63) >> 1);
}
__device__ __forceinline__ bf16x8 tr_frag(const char* lds_lo, const char* lds_hi) {
  v4s lo = __builtin_amdgcn_ds_read_tr16_b64_v4i16((__attribute__((address_space(3))) v4s*)(lds_lo));
  v4s hi = __builtin_amdgcn_ds_read_tr16_b64_v4i16((__attribute__((address_space(3))) v4s*)(lds_hi));
  bf16x8 r;
  r[0] = lo[0]; r[1] = lo[1]; r[2] = lo[2]; r[3] = lo[3];
  r[4] = hi[0]; r[5] = hi[1]; r[6] = hi[2]; r[7] = hi[3];
  return r;
}
typedef __attribute__((ext_vector_type(2))) unsigned u32x2;
typedef __attribute__((ext_vector_type(4))) unsigned u32x4;
template <int OFF> __device__ __forceinline__ u32x2 lds_tr64(unsigned addr) {
  u32x2 r; asm volatile("ds_read_b64_tr_b16 %0, %1 offset:%2" : "=v"(r) : "v"(addr), "i"(OFF) : "memory"); return r;
}
template <int OFF> __device__ __forceinline__ bf16x8 lds_b128(unsigned addr) {
  u32x4 r; asm volatile("ds_read_b128 %0, %1 offset:%2" : "=v"(r) : "v"(addr), "i"(OFF) : "memory"); return __builtin_bit_cast(bf16x8, r);
}
__device__ __forceinline__ bf16x8 join_frag(u32x2 lo, u32x2 hi) {
  u32x4 r = __builtin_shufflevector(lo, hi, 0, 1, 2, 3); return __builtin_bit_cast(bf16x8, r);
}
#define WAIT_LGKM(n) do { asm volatile("s_waitcnt lgkmcnt(" #n ")" ::: "memory"); __builtin_amdgcn_sched_barrier(0); } while (0)
#define GLDS16(gptr, ldsptr) __builtin_amdgcn_global_load_lds((const void __attribute__((address_space(1)))*)(gptr), (void __attribute__((address_space(3)))*)(ldsptr), 16, 0, 0)

__device__ __forceinline__ float xor8(float v) {
  return __int_as_float(__builtin_amdgcn_mov_dpp(__float_as_int(v), 0x128, 0xf, 0xf, false));
}
template <int MASK> __device__ __forceinline__ float sel_xor8(float old, float src) {
  return __int_as_float(__builtin_amdgcn_update_dpp(__float_as_int(old), __float_as_int(src), 0x128, 0xf, MASK, false));
}
__device__ __forceinline__ float gelu_tanh_f(float x) {
  const float c1 = -2.0f * 0.7978845608028654f * 1.4426950408889634f, c3 = c1 * 0.044715f;
  const float e = __builtin_amdgcn_exp2f(x * __builtin_fmaf(c3, x * x, c1));
  return x * __builtin_amdgcn_rcpf(1.0f + e);
}

constexpr int G1_BM = 256, G1_BN = 288, G1_CH = G1_BN / 8;
constexpr int G1_BK = 32, G1_NST = 4, G1_NK = 1024 / G1_BK;
constexpr int G1_A_BYTES = G1_BM * G1_BK * 2, G1_B_BYTES = G1_BK * G1_BN * 2, G1_STAGE = G1_A_BYTES + G1_B_BYTES;
__device__ __forceinline__ int g1_swz(int k) { return ((k >> 3) & 1) << 1; }
#define WAIT_VM(n) asm volatile("s_waitcnt vmcnt(" #n ") lgkmcnt(0)" ::: "memory")
#define RAW_BAR() __builtin_amdgcn_s_barrier()

__global__ __launch_bounds__(512, 2) void gemm1_kernel(
    const bf16* __restrict__ wg, const bf16* __restrict__ wu, char* __restrict__ ws) {
  extern __shared__ __attribute__((aligned(16))) char smem[];
  const int tid = threadIdx.x, lane = tid & 63, wid = __builtin_amdgcn_readfirstlane(tid >> 6);
  const int bid = blockIdx.x;
  const int xcd = bid & 7, idx = bid >> 3;
  const int pn = xcd * 4 + (idx & 3), pm = idx >> 2;
  const int brow = pm * G1_BM;
  const int wr = wid & 3, wc = wid >> 2;
  const unsigned short* xn = (const unsigned short*)(ws + OFF_XN);
  const unsigned short* bt = (const unsigned short*)(ws + OFF_BT);

  unsigned aoffs[2];
#pragma unroll
  for (int i = 0; i < 2; ++i) {
    int sb = lane * 16, swz = sb ^ (((sb >> 9) & 1) << 5);
    int R = (i * 8 + wid) * 16 + (swz >> 6), C = (swz & 63) >> 1;
    aoffs[i] = (unsigned)((brow + R) * LDXN + C);
  }
  const unsigned short* bsrc[3];
#pragma unroll
  for (int i = 0; i < 3; ++i) {
    int inst = i * 8 + wid; if (inst > 17) inst = 17;
    int Lc = inst * 64 + lane, k = Lc / G1_CH, cp = Lc % G1_CH, c = cp ^ g1_swz(k);
    int half = c & 1, blk = c >> 1, w_ = blk >= 9, jj = blk - w_ * 9;
    const unsigned short* base = (const unsigned short*)(half ? wu : wg);
    int o = pn * 112 + w_ * 56 + jj * 8;
    if (jj >= 7) { base = bt; o = half * 1024 + pn * 32 + w_ * 16 + (jj - 7) * 8; }
    bsrc[i] = base + (k * LDW + o);
  }
#define G1_STAGE_LOADS(buf) do { \
    char* sa_ = smem + (buf) * G1_STAGE; char* sb_ = sa_ + G1_A_BYTES; \
    _Pragma("unroll") for (int i = 0; i < 2; ++i) { GLDS16(xn + aoffs[i], sa_ + (i * 8 + wid) * 1024); aoffs[i] += G1_BK; } \
    _Pragma("unroll") for (int i = 0; i < 2; ++i) { GLDS16(bsrc[i], sb_ + (i * 8 + wid) * 1024); bsrc[i] += G1_BK * LDW; } \
    if (wid < 2) { GLDS16(bsrc[2], sb_ + (16 + wid) * 1024); bsrc[2] += G1_BK * LDW; } \
  } while (0)
  f32x4 acc[4][9];
#pragma unroll
  for (int i = 0; i < 4; ++i)
#pragma unroll
    for (int j = 0; j < 9; ++j) acc[i][j] = (f32x4){0.f, 0.f, 0.f, 0.f};

  const int fr = lane & 15, fq = lane >> 4, tq = (lane & 15) >> 2, tp = lane & 3;
  const int ob_ = fr * 64 + fq * 16, aoff0 = (wr * 4) * 1024 + (ob_ ^ (((ob_ >> 9) & 1) << 5));
  const int bRow = G1_A_BYTES + ((8 * fq + tq) * G1_CH + (tp >> 1)) * 16 + 8 * (tp & 1) + 32 * 9 * wc;
  const int sgn = 32 * (fq & 1);
  const unsigned ldsb = (unsigned)(size_t)(__attribute__((address_space(3))) char*)smem;
  const unsigned aA = ldsb + aoff0;
  const unsigned aE = ldsb + bRow + (wc ? -sgn : sgn);
  const unsigned aO = ldsb + bRow + (wc ? sgn : -sgn);
#define G1_RDB1(jj, ad) join_frag(lds_tr64<32 * (jj)>((ad) + bo_), lds_tr64<32 * (jj) + 4 * G1_CH * 16>((ad) + bo_))
#define G1_READ(buf) do { \
    const unsigned bo_ = (buf) * G1_STAGE; \
    bfr[0] = G1_RDB1(0, aE); bfr[1] = G1_RDB1(1, aO); bfr[2] = G1_RDB1(2, aE); \
    af[0] = lds_b128<0>(aA + bo_); af[1] = lds_b128<1024>(aA + bo_); af[2] = lds_b128<2048>(aA + bo_); af[3] = lds_b128<3072>(aA + bo_); \
    bfr[3] = G1_RDB1(3, aO); bfr[4] = G1_RDB1(4, aE); bfr[5] = G1_RDB1(5, aO); \
    bfr[6] = G1_RDB1(6, aE); bfr[7] = G1_RDB1(7, aO); bfr[8] = G1_RDB1(8, aE); \
  } while (0)
#define G1_MMA() do { \
    __builtin_amdgcn_s_setprio(1); \
    _Pragma("unroll") for (int jj = 0; jj < 9; ++jj) \
      _Pragma("unroll") for (int mi = 0; mi < 4; ++mi) \
        acc[mi][jj] = __builtin_amdgcn_mfma_f32_16x16x32_bf16(af[mi], bfr[jj], acc[mi][jj], 0, 0, 0); \
    __builtin_amdgcn_s_setprio(0); \
  } while (0)
#define G1_WAITN(n2, n1) do { if (wid < 2) WAIT_VM(n2); else WAIT_VM(n1); __builtin_amdgcn_sched_barrier(0); } while (0)
#define G1_STEP(u, LOADS, w2, w1) do { \
    G1_READ(u); \
    LOADS; \
    G1_WAITN(w2, w1); RAW_BAR(); \
    G1_MMA(); \
    G1_WAITN(w2, w1); RAW_BAR(); \
  } while (0)

  bf16x8 af[4], bfr[9];
  G1_STAGE_LOADS(0); G1_STAGE_LOADS(1); G1_STAGE_LOADS(2);
  G1_WAITN(10, 8); RAW_BAR();
  if (wc == 1) RAW_BAR();
  for (int kt0 = 0; kt0 < G1_NK - 4; kt0 += 4) {
    G1_STEP(0, G1_STAGE_LOADS(3), 10, 8);
    G1_STEP(1, G1_STAGE_LOADS(0), 10, 8);
    G1_STEP(2, G1_STAGE_LOADS(1), 10, 8);
    G1_STEP(3, G1_STAGE_LOADS(2), 10, 8);
  }
  G1_STEP(0, G1_STAGE_LOADS(3), 10, 8);
  G1_STEP(1, , 5, 4);
  G1_STEP(2, , 0, 0);
  G1_STEP(3, , 0, 0);
  if (wc == 0) RAW_BAR();

  int tz = threadIdx.x; asm volatile("" : "+v"(tz));
  const int lane2 = tz & 63, fr2 = tz & 15, fq2 = (tz >> 4) & 3;
  const bool hi = fr2 >= 8;
  const int f8 = fr2 & 7, rb = fq2 * 4 + (hi ? 2 : 0);
  unsigned short* ah = (unsigned short*)(ws + OFF_AH);
  float2* bu = (float2*)(ws + OFF_BU);
  const float2* coef = (const float2*)(ws + OFF_COEF);
  const float2* lamb = (const float2*)(ws + OFF_LAM);
  float2* Eo = (float2*)(ws + OFF_E);
  const int P0 = pn * 112 + wc * 56;
  const int S0 = pn * 32 + wc * 16;
  constexpr int nf = 7;
  RAW_BAR();
  constexpr int EP_LD = 160;
  char* wl = smem + wid * (64 * EP_LD);
#pragma unroll
  for (int jj = 0; jj < 9; ++jj) {
    if (jj < nf) {
#pragma unroll
      for (int mi = 0; mi < 4; ++mi) {
        const f32x4 v = acc[mi][jj];
        const float g0 = rbf(sel_xor8<0xC>(v[0], v[2])), g1 = rbf(sel_xor8<0xC>(v[1], v[3]));
        const float u0 = rbf(sel_xor8<0x3>(v[2], v[0])), u1 = rbf(sel_xor8<0x3>(v[3], v[1]));
        char* d = wl + (mi * 16 + rb) * EP_LD + (jj * 8 + f8) * 2;
        *(unsigned short*)(d) = f2bf(rbf(gelu_tanh_f(g0)) * u0);
        *(unsigned short*)(d + EP_LD) = f2bf(rbf(gelu_tanh_f(g1)) * u1);
      }
    } else {
      const int p = S0 + (jj - 7) * 8 + f8;
      const float2 cf = coef[p];
      const float2 lam = lamb[p];
      const float l2r = lam.x * lam.x - lam.y * lam.y, l2i = 2.f * lam.x * lam.y;
      const float l4r = l2r * l2r - l2i * l2i, l4i = 2.f * l2r * l2i;
      const float l8r = l4r * l4r - l4i * l4i, l8i = 2.f * l4r * l4i;
      const float l16r = l8r * l8r - l8i * l8i, l16i = 2.f * l8r * l8i;
      float mr = (fq2 & 1) ? 1.f : l4r, mim = (fq2 & 1) ? 0.f : l4i;
      if (!(fq2 & 2)) { const float a_ = mr * l8r - mim * l8i, b_ = mr * l8i + mim * l8r; mr = a_; mim = b_; }
      if (!hi) { const float a_ = mr * l2r - mim * l2i, b_ = mr * l2i + mim * l2r; mr = a_; mim = b_; }
      float er[4], ei[4];
#pragma unroll
      for (int mi = 0; mi < 4; ++mi) {
        const f32x4 v = acc[mi][jj];
        const float ur0 = sel_xor8<0xC>(v[0], v[2]), ur1 = sel_xor8<0xC>(v[1], v[3]);
        const float ui0 = sel_xor8<0x3>(v[2], v[0]), ui1 = sel_xor8<0x3>(v[3], v[1]);
        const float b0r = cf.x * ur0 - cf.y * ui0, b0i = cf.x * ui0 + cf.y * ur0;
        const float b1r = cf.x * ur1 - cf.y * ui1, b1i = cf.x * ui1 + cf.y * ur1;
        const int row = brow + wr * 64 + mi * 16 + rb;
        bu[(size_t)row * PP + p] = make_float2(b0r, b0i);
        bu[(size_t)(row + 1) * PP + p] = make_float2(b1r, b1i);
        er[mi] = lam.x * b0r - lam.y * b0i + b1r; ei[mi] = lam.x * b0i + lam.y * b0r + b1i;
      }
#pragma unroll
      for (int ch = 0; ch < 2; ++ch) {
        const float tr_ = l16r * er[2 * ch] - l16i * ei[2 * ch] + er[2 * ch + 1], ti_ = l16r * ei[2 * ch] + l16i * er[2 * ch] + ei[2 * ch + 1];
        float sr = mr * tr_ - mim * ti_, si = mr * ti_ + mim * tr_;
        sr += xor8(sr); si += xor8(si);
        sr += __shfl_xor(sr, 16); si += __shfl_xor(si, 16);
        sr += __shfl_xor(sr, 32); si += __shfl_xor(si, 32);
        if (lane2 < 8) Eo[(size_t)(((brow + wr * 64) >> 5) + ch) * PP + p] = make_float2(sr, si);
      }
    }
  }
  asm volatile("s_waitcnt lgkmcnt(0)" ::: "memory");
  {
    unsigned short* dst0 = ah + (size_t)(brow + wr * 64) * KAH + P0;
#pragma unroll
    for (int i = 0; i < 7; ++i) {
      const int q = i * 64 + lane2, row = q / 7, ch = q % 7;
      {
        bf16x8 v = *(const bf16x8*)(wl + row * EP_LD + ch * 16);
        *(bf16x8*)(dst0 + (size_t)row * KAH + ch * 8) = v;
      }
    }
  }
}

__global__ __launch_bounds__(256) void scanC_kernel(char* __restrict__ ws, const bf16* __restrict__ wd) {
  if (blockIdx.x >= 4 * NCHUNK) {
    const bf16x8* src = (const bf16x8*)wd + (size_t)(blockIdx.x - 4 * NCHUNK) * 1024 + threadIdx.x;
    bf16x8 v0 = src[0], v1 = src[256], v2 = src[512], v3 = src[768];
    asm volatile("" :: "v"(v0), "v"(v1), "v"(v2), "v"(v3));
    return;
  }
  const int p = (blockIdx.x & 3) * 256 + threadIdx.x, c = blockIdx.x >> 2;
  const float2* bu = (const float2*)(ws + OFF_BU) + (size_t)c * SCAN_T * PP + p;
  float2 b[SCAN_T];
#pragma unroll
  for (int s = 0; s < SCAN_T; ++s) b[s] = bu[(size_t)s * PP];
  const float2 lam = ((const float2*)(ws + OFF_LAM))[p];
  const float2* E = (const float2*)(ws + OFF_E) + p;
  float lr = lam.x, li = lam.y;
#pragma unroll
  for (int i = 0; i < 5; ++i) { float a = lr * lr - li * li, bb = 2.f * lr * li; lr = a; li = bb; }
  float hr = 0.f, hi = 0.f;
  {
    float2 e[NCHUNK - 1];
#pragma unroll
    for (int u = 0; u < NCHUNK - 1; ++u) e[u] = (u < c) ? E[(size_t)u * PP] : make_float2(0.f, 0.f);
#pragma unroll
    for (int u = 0; u < NCHUNK - 1; ++u) if (u < c) { float nr = lr * hr - li * hi + e[u].x, ni = lr * hi + li * hr + e[u].y; hr = nr; hi = ni; }
  }
  unsigned short* ah = (unsigned short*)(ws + OFF_AH) + (size_t)c * SCAN_T * KAH + FF + p;
#pragma unroll
  for (int s = 0; s < SCAN_T; ++s) {
    float nr = lam.x * hr - lam.y * hi + b[s].x, ni = lam.x * hi + lam.y * hr + b[s].y;
    hr = nr; hi = ni;
    ah[(size_t)s * KAH] = f2bf(hr);
    ah[(size_t)s * KAH + PP] = f2bf(hi);
  }
}

constexpr int G2_BM = 128, G2_BN = 64, G2_CH = G2_BN / 8;
constexpr int G2_A_BYTES = G2_BM * 64 * 2, G2_B_BYTES = 64 * G2_BN * 2, G2_STAGE = G2_A_BYTES + G2_B_BYTES;
constexpr int G2_NST = 5;
__device__ __forceinline__ int g2_swz(int k) { return (((k >> 1) & 1) << 1) | (((k >> 3) & 1) << 2); }

__global__ __launch_bounds__(256) void gemm2_kernel(
    const bf16* __restrict__ x, const bf16* __restrict__ wd, const bf16* __restrict__ dskip,
    const char* __restrict__ ws, float* __restrict__ out) {
  extern __shared__ __attribute__((aligned(16))) char smem[];
  const int tid = threadIdx.x, lane = tid & 63, wid = __builtin_amdgcn_readfirstlane(tid >> 6);
  const int bid = blockIdx.x;
  const int xcd = bid & 7, idx = bid >> 3;
  const int pm = (xcd & 3) * 4 + (idx & 3), pn = (xcd >> 2) * 8 + (idx >> 2);
  const int brow = pm * G2_BM, bcol = pn * G2_BN;
  const unsigned short* ah = (const unsigned short*)(ws + OFF_AH);
  const unsigned short* ct = (const unsigned short*)(ws + OFF_CT);
  const unsigned short* asrc[4];
#pragma unroll
  for (int i = 0; i < 4; ++i) {
    int R = (i * 4 + wid) * 8 + (lane >> 3), C = ((lane & 7) ^ (R & 7)) * 8;
    asrc[i] = ah + (size_t)(brow + R) * KAH + C;
  }
  int boffs[2];
#pragma unroll
  for (int i = 0; i < 2; ++i) {
    int Lc = (i * 4 + wid) * 64 + lane, k = Lc / G2_CH, cp = Lc % G2_CH, c = cp ^ g2_swz(k);
    boffs[i] = k * DM + bcol + c * 8;
  }
#define G2_STAGE_LOADS(buf, kt) do { \
    char* sa_ = smem + (buf) * G2_STAGE; char* sb_ = sa_ + G2_A_BYTES; \
    _Pragma("unroll") for (int i = 0; i < 4; ++i) GLDS16(asrc[i] + (kt) * 64, sa_ + (i * 4 + wid) * 1024); \
    const unsigned short* bb_ = (kt) < 56 ? (const unsigned short*)wd + (size_t)(kt) * 64 * DM : ct + (size_t)((kt) - 56) * 64 * DM; \
    _Pragma("unroll") for (int i = 0; i < 2; ++i) GLDS16(bb_ + boffs[i], sb_ + (i * 4 + wid) * 1024); \
  } while (0)
  const int wr = wid >> 1, wc = wid & 1;
  const int fr = lane & 15, fq = lane >> 4, tq = (lane & 15) >> 2, tp = lane & 3;
  f32x4 accd[4][2], accy[4][2];
#pragma unroll
  for (int i = 0; i < 4; ++i)
#pragma unroll
    for (int j = 0; j < 2; ++j) { accd[i][j] = (f32x4){0.f, 0.f, 0.f, 0.f}; accy[i][j] = (f32x4){0.f, 0.f, 0.f, 0.f}; }

  const unsigned ldsb = (unsigned)(size_t)(__attribute__((address_space(3))) char*)smem;
  const unsigned aA0 = ldsb + (wr * 64 + fr) * 128 + ((fq ^ (fr & 7)) * 16);
  const unsigned aA1 = ldsb + (wr * 64 + fr) * 128 + (((4 + fq) ^ (fr & 7)) * 16);
  unsigned aB[2][2];
#pragma unroll
  for (int nj = 0; nj < 2; ++nj)
#pragma unroll
    for (int t = 0; t < 2; ++t) {
      const int k0 = 8 * fq + tq + 4 * t, j = wc * 2 + nj;
      aB[nj][t] = ldsb + G2_A_BYTES + (k0 * G2_CH + ((2 * j + (tp >> 1)) ^ g2_swz(k0))) * 16 + 8 * (tp & 1);
    }
#define G2_RD(af, bfm, buf, kk) do { \
    bfm[0] = join_frag(lds_tr64<(kk) * 32 * G2_CH * 16>(aB[0][0] + (buf) * G2_STAGE), lds_tr64<(kk) * 32 * G2_CH * 16>(aB[0][1] + (buf) * G2_STAGE)); \
    bfm[1] = join_frag(lds_tr64<(kk) * 32 * G2_CH * 16>(aB[1][0] + (buf) * G2_STAGE), lds_tr64<(kk) * 32 * G2_CH * 16>(aB[1][1] + (buf) * G2_STAGE)); \
    af[0] = lds_b128<0>(((kk) ? aA1 : aA0) + (buf) * G2_STAGE); af[1] = lds_b128<2048>(((kk) ? aA1 : aA0) + (buf) * G2_STAGE); \
    af[2] = lds_b128<4096>(((kk) ? aA1 : aA0) + (buf) * G2_STAGE); af[3] = lds_b128<6144>(((kk) ? aA1 : aA0) + (buf) * G2_STAGE); \
  } while (0)
#define G2_MMA(acc, af, bfm) do { \
    _Pragma("unroll") for (int mi = 0; mi < 4; ++mi) \
      _Pragma("unroll") for (int nj = 0; nj < 2; ++nj) acc[mi][nj] = __builtin_amdgcn_mfma_f32_16x16x32_bf16(af[mi], bfm[nj], acc[mi][nj], 0, 0, 0); \
  } while (0)
#define compute(buf, acc) do { \
    bf16x8 a0[4], a1[4], f0[2], f1[2]; \
    G2_RD(a0, f0, buf, 0); \
    WAIT_LGKM(0); \
    G2_RD(a1, f1, buf, 1); \
    G2_MMA(acc, a0, f0); \
    WAIT_LGKM(0); \
    G2_MMA(acc, a1, f1); \
  } while (0)
  G2_STAGE_LOADS(0, 0); G2_STAGE_LOADS(1, 1); G2_STAGE_LOADS(2, 2); G2_STAGE_LOADS(3, 3);
#define G2_STEP(u, kt, acc) do { WAIT_VM(18); RAW_BAR(); G2_STAGE_LOADS(((u) + 4) % 5, (kt) + 4); compute(u, acc); } while (0)
  for (int kt0 = 0; kt0 < 55; kt0 += 5) {
    G2_STEP(0, kt0, accd); G2_STEP(1, kt0 + 1, accd); G2_STEP(2, kt0 + 2, accd); G2_STEP(3, kt0 + 3, accd); G2_STEP(4, kt0 + 4, accd);
  }
  WAIT_VM(18); RAW_BAR(); G2_STAGE_LOADS(4, 59); compute(0, accd);
  for (int kt0 = 56; kt0 < 81; kt0 += 5) {
    G2_STEP(1, kt0, accy); G2_STEP(2, kt0 + 1, accy); G2_STEP(3, kt0 + 2, accy); G2_STEP(4, kt0 + 3, accy); G2_STEP(0, kt0 + 4, accy);
  }
  WAIT_VM(18); RAW_BAR(); G2_STAGE_LOADS(0, 85); compute(1, accy);
  WAIT_VM(18); RAW_BAR(); G2_STAGE_LOADS(1, 86); compute(2, accy);
  WAIT_VM(18); RAW_BAR(); G2_STAGE_LOADS(2, 87); compute(3, accy);
  WAIT_VM(18); RAW_BAR(); compute(4, accy);
  WAIT_VM(12); RAW_BAR(); compute(0, accy);
  WAIT_VM(6); RAW_BAR(); compute(1, accy);
  WAIT_VM(0); RAW_BAR(); compute(2, accy);
  const unsigned short* xs = (const unsigned short*)x;
  const unsigned short* xn = (const unsigned short*)(ws + OFF_XN);
  WAIT_VM(0); RAW_BAR();
  constexpr int EP_LD = 32 * 8 + 16;
  char* wl = smem + wid * (64 * EP_LD);
#pragma unroll
  for (int nj = 0; nj < 2; ++nj)
#pragma unroll
    for (int mi = 0; mi < 4; ++mi)
#pragma unroll
      for (int r = 0; r < 4; ++r) {
        const float dn = rbf(accd[mi][nj][r]);
        const float g = rbf(dn * __builtin_amdgcn_rcpf(1.0f + __expf(-dn)));
        *(float2*)(wl + (mi * 16 + fq * 4 + r) * EP_LD + (nj * 16 + fr) * 8) = make_float2(2.0f * accy[mi][nj][r], g);
      }
  asm volatile("s_waitcnt lgkmcnt(0)" ::: "memory");
  {
    const int c4 = (lane & 7) * 4, rsub = lane >> 3;
    const int col = bcol + wc * 32 + c4;
    const unsigned short* dk = (const unsigned short*)dskip + col;
    const bf16x4 dk4 = *(const bf16x4*)dk;
#pragma unroll
    for (int i = 0; i < 8; ++i) {
      const int rl = i * 8 + rsub, row = brow + wr * 64 + rl;
      const size_t o = (size_t)row * DM + col;
      const bf16x4 x4 = *(const bf16x4*)(xs + o), n4 = *(const bf16x4*)(xn + (size_t)row * LDXN + col);
      const f32x4 p0 = *(const f32x4*)(wl + rl * EP_LD + c4 * 8), p1 = *(const f32x4*)(wl + rl * EP_LD + c4 * 8 + 16);
      f32x4 res;
      res[0] = bf2f((unsigned short)x4[0]) + (p0[0] + rbf(bf2f((unsigned short)n4[0]) * bf2f((unsigned short)dk4[0]))) * p0[1];
      res[1] = bf2f((unsigned short)x4[1]) + (p0[2] + rbf(bf2f((unsigned short)n4[1]) * bf2f((unsigned short)dk4[1]))) * p0[3];
      res[2] = bf2f((unsigned short)x4[2]) + (p1[0] + rbf(bf2f((unsigned short)n4[2]) * bf2f((unsigned short)dk4[2]))) * p1[1];
      res[3] = bf2f((unsigned short)x4[3]) + (p1[2] + rbf(bf2f((unsigned short)n4[3]) * bf2f((unsigned short)dk4[3]))) * p1[3];
      *(f32x4*)(out + o) = res;
    }
  }
}

extern "C" void kernel_launch(void* const* d_in, const int* in_sizes, int n_in,
                              void* d_out, int out_size, void* d_ws, size_t ws_size,
                              hipStream_t stream) {
  const bf16* x = (const bf16*)d_in[0];
  const bf16* lam_re = (const bf16*)d_in[1];
  const bf16* lam_im = (const bf16*)d_in[2];
  const bf16* b_re = (const bf16*)d_in[3];
  const bf16* b_im = (const bf16*)d_in[4];
  const bf16* c_re = (const bf16*)d_in[5];
  const bf16* c_im = (const bf16*)d_in[6];
  const bf16* log_dt = (const bf16*)d_in[7];
  const bf16* dskip = (const bf16*)d_in[8];
  const bf16* wg = (const bf16*)d_in[10];
  const bf16* wu = (const bf16*)d_in[11];
  const bf16* wd = (const bf16*)d_in[12];
  char* ws = (char*)d_ws;
  float* out = (float*)d_out;

  hipFuncSetAttribute((const void*)gemm1_kernel, hipFuncAttributeMaxDynamicSharedMemorySize, G1_NST * G1_STAGE);
  hipFuncSetAttribute((const void*)gemm2_kernel, hipFuncAttributeMaxDynamicSharedMemorySize, G2_NST * G2_STAGE);

  prep_kernel<<<PREP_BLOCKS, 256, 0, stream>>>(x, lam_re, lam_im, b_re, b_im, c_re, c_im, log_dt, ws, wg, wu);
  gemm1_kernel<<<256, 512, G1_NST * G1_STAGE, stream>>>(wg, wu, ws);
  scanC_kernel<<<4 * NCHUNK + 448, 256, 0, stream>>>(ws, wd);
  gemm2_kernel<<<256, 256, G2_NST * G2_STAGE, stream>>>(x, wd, dskip, ws, out);
}
```
